```python
import torch

def _softmax(x, axis=-1):
    return torch.softmax(x, dim=axis)
def _gelu(x):
    return torch.nn.functional.gelu(x, approximate="tanh")
def _silu(x):
    return torch.nn.functional.silu(x)
def _sigmoid(x):
    return torch.sigmoid(x)
def _layernorm(x, axis=-1, eps=1e-5):
    m = x.mean(dim=axis, keepdim=True)
    v = x.var(dim=axis, keepdim=True, unbiased=False)
    return (x - m) / torch.sqrt(v + eps)
def _scatter_add(tgt, idx, src, axis=0):
    return tgt.clone().index_add_(axis, idx, src)
def _segment_sum(src, seg, n):
    out = torch.zeros((n,) + src.shape[1:], dtype=src.dtype)
    return out.index_add_(0, seg, src)
def _pad(x, pw):
    flat = tuple(v for pair in reversed(pw) for v in pair)
    return torch.nn.functional.pad(x, flat)

kernel_name = 'standalone__self_sup_conv_stem_hubert__speech'

def setup_inputs():

    _w = (lambda a: (lambda t: t.to(torch.bfloat16) if t.is_floating_point() else t)(torch.as_tensor(a)))
    B, T, D, K = 2, 4000, 256, 31
    scale = (1.0 / (K * D)) ** 0.5
    return {
        "x": _w((lambda *s: torch.randn(*s, dtype=torch.bfloat16) * 0.05)(B, T, D)),
        "w": _w((lambda *s: torch.randn(*s, dtype=torch.bfloat16) * 0.05)(K, D, D) * scale),
        "b": _w((lambda *s: torch.randn(*s, dtype=torch.bfloat16) * 0.05)(D) * 0.1),
        "gamma": _w(torch.ones((D,), dtype=float)),
        "beta": _w(torch.zeros((D,), dtype=float)),
    }

def reference(x, w, b, gamma, beta):
    B, T, D = x.shape
    K = w.shape[0]
    s = 2
    L = (T - K) // s + 1
    y = torch.einsum('blc,cd->bld', x[:, 0 : s * L : s, :], w[0])
    for i in range(1, K):
        y = y + torch.einsum('blc,cd->bld', x[:, i : i + s * L : s, :], w[i])
    y = y + b[None, None, :]
    mu = y.mean(1, keepdims=True)
    var = ((y - mu) ** 2).mean(1, keepdims=True)
    yn = (y - mu) / torch.sqrt(var + 1e-5)
    yn = yn * gamma[None, None, :] + beta[None, None, :]
    return _gelu(yn)
```

```cpp
#include <hip/hip_runtime.h>
#include <hip/hip_bf16.h>

typedef __attribute__((ext_vector_type(8))) short bf16x8;
typedef __attribute__((ext_vector_type(4))) short bf16x4;
typedef __attribute__((ext_vector_type(16))) float f32x16;
typedef __attribute__((address_space(3))) bf16x4 lds_bf16x4;

constexpr int T_ = 4000, D_ = 256, KT = 31, L_ = 1985;
constexpr int BM = 128, BN = 32;
constexpr int MT = 16;
constexpr int XR = 144;
constexpr int XBLK = 2 * XR * 128;
constexpr int WST = 4096;
constexpr int RW = 8;
constexpr int PD = 7;
constexpr int NST = 4 * KT;
constexpr int LDS_BYTES = 2 * XBLK + RW * WST;
constexpr int NWB = 64;

__global__ __launch_bounds__(256, 1)
void conv_gemm_kernel(const __hip_bfloat16* __restrict__ x, const __hip_bfloat16* __restrict__ w,
                      const __hip_bfloat16* __restrict__ bias, float* __restrict__ y,
                      float2* __restrict__ part) {
  extern __shared__ __attribute__((aligned(16))) char sm[];
  const int tid = threadIdx.x, lane = tid & 63, wid = tid >> 6;
  const int bid = blockIdx.x;
  const int nt = bid & 7, rest = bid >> 3;
  const int b = rest >> 4, mt = rest & 15;
  const int t0 = 2 * mt * BM;
  const __hip_bfloat16* xb = x + (size_t)b * T_ * D_;

  auto load_x_piece = [&](int cb, int q) {
    int p = q / 18, rb = q % 18;
    int rl = rb * 8 + (lane >> 3);
    int ch = (lane & 7) ^ ((rl >> 1) & 7);
    int xrow = t0 + 2 * rl + p; xrow = xrow < T_ ? xrow : T_ - 1;
    __builtin_amdgcn_global_load_lds((const unsigned*)(xb + (size_t)xrow * D_ + cb * 64 + ch * 8),
        (unsigned*)(sm + (cb & 1) * XBLK + q * 1024), 16, 0, 0);
  };
  auto load_w = [&](int s) {
    int cb = s / KT, tap = s - cb * KT;
    int row = wid * 16 + (lane >> 2);
    __builtin_amdgcn_global_load_lds((const unsigned*)(w + ((size_t)(tap * 256 + cb * 64 + row)) * D_ + nt * BN + (lane & 3) * 8),
        (unsigned*)(sm + 2 * XBLK + (s % RW) * WST + wid * 1024), 16, 0, 0);
  };

  for (int q = wid; q < 36; q += 4) load_x_piece(0, q);
  #pragma unroll
  for (int s = 0; s < PD; ++s) load_w(s);

  f32x16 acc = {};
  const int r = lane & 31, h = lane >> 5;
  const int troff = (8 * h + ((lane & 15) >> 2)) * 64 + (((lane >> 4) & 1) * 16 + (lane & 3) * 4) * 2;
  const int arow0 = wid * 32 + r;

  for (int s = 0; s < NST; ++s) {
    const int cb = s / KT, tap = s - cb * KT;
    if (s + PD - 1 < NST) asm volatile("s_waitcnt vmcnt(6)" ::: "memory");
    else asm volatile("s_waitcnt vmcnt(0)" ::: "memory");
    __builtin_amdgcn_s_barrier();
    if (s + PD < NST) load_w(s + PD);
    if (tap < 9 && cb < 3) load_x_piece(cb + 1, wid + 4 * tap);
    const int j = tap >> 1, p = tap & 1;
    const int ar = arow0 + j;
    const char* abase = sm + (cb & 1) * XBLK + p * (XR * 128) + ar * 128;
    const int sw = (ar >> 1) & 7;
    const char* wbase = sm + 2 * XBLK + (s % RW) * WST + troff;
    #pragma unroll
    for (int kc = 0; kc < 4; ++kc) {
      bf16x8 a = *(const bf16x8*)(abase + (((2 * kc + h) ^ sw) << 4));
      bf16x4 b0 = __builtin_amdgcn_ds_read_tr16_b64_v4i16((lds_bf16x4*)(wbase + kc * 1024));
      bf16x4 b1 = __builtin_amdgcn_ds_read_tr16_b64_v4i16((lds_bf16x4*)(wbase + kc * 1024 + 256));
      bf16x8 bb = __builtin_shufflevector(b0, b1, 0, 1, 2, 3, 4, 5, 6, 7);
      acc = __builtin_amdgcn_mfma_f32_32x32x16_bf16(a, bb, acc, 0, 0, 0);
    }
  }

  const int col = nt * BN + r;
  const float bv = __bfloat162float(bias[col]);
  const int row0 = mt * BM + wid * 32;
  int cnt = L_ - row0; cnt = cnt > 32 ? 32 : cnt;
  float sum = 0.f;
  #pragma unroll
  for (int i = 0; i < 16; ++i) {
    int rr = (i & 3) + 8 * (i >> 2) + 4 * h;
    float v = acc[i] + bv;
    acc[i] = v;
    if (rr < cnt) { sum += v; y[((size_t)b * L_ + row0 + rr) * D_ + col] = v; }
  }
  sum += __shfl_xor(sum, 32);
  if (cnt > 0) {
    float mean = sum / (float)cnt;
    float m2 = 0.f;
    #pragma unroll
    for (int i = 0; i < 16; ++i) {
      int rr = (i & 3) + 8 * (i >> 2) + 4 * h;
      float d = acc[i] - mean;
      if (rr < cnt) m2 += d * d;
    }
    m2 += __shfl_xor(m2, 32);
    if (h == 0) part[((size_t)b * NWB + mt * 4 + wid) * D_ + col] = make_float2(mean, m2);
  }
}

__global__ __launch_bounds__(256)
void norm_gelu_kernel(const float* __restrict__ y, const float2* __restrict__ part,
                      const __hip_bfloat16* __restrict__ gamma, const __hip_bfloat16* __restrict__ beta,
                      __hip_bfloat16* __restrict__ out) {
  __shared__ float s_scale[256], s_shift[256];
  const int tid = threadIdx.x;
  const int b = blockIdx.y;
  {
    float n = 0.f, mean = 0.f, m2 = 0.f;
    for (int i = 0; i < 63; ++i) {
      float2 pm = part[((size_t)b * NWB + i) * D_ + tid];
      float c = (i < 62) ? 32.f : 1.f;
      float nn = n + c;
      float d = pm.x - mean;
      mean += d * (c / nn);
      m2 += pm.y + d * d * (n * c / nn);
      n = nn;
    }
    float var = m2 / n;
    float rs = rsqrtf(var + 1e-5f);
    float g = __bfloat162float(gamma[tid]), be = __bfloat162float(beta[tid]);
    s_scale[tid] = rs * g;
    s_shift[tid] = be - mean * rs * g;
  }
  __syncthreads();
  const int rows_per = 8;
  const int r0 = blockIdx.x * rows_per;
  const int c4 = (tid & 63) * 4;
  for (int rr = r0 + (tid >> 6); rr < r0 + rows_per && rr < L_; rr += 4) {
    float4 v = *(const float4*)(y + ((size_t)b * L_ + rr) * D_ + c4);
    float o[4] = {v.x, v.y, v.z, v.w};
    ushort4 ov;
    unsigned short* op = (unsigned short*)&ov;
    #pragma unroll
    for (int i = 0; i < 4; ++i) {
      float z = o[i] * s_scale[c4 + i] + s_shift[c4 + i];
      float u = 0.7978845608028654f * (z + 0.044715f * z * z * z);
      float t = tanhf(u);
      float g = 0.5f * z * (1.f + t);
      __hip_bfloat16 hb = __float2bfloat16(g);
      op[i] = *(unsigned short*)&hb;
    }
    *(ushort4*)(out + ((size_t)b * L_ + rr) * D_ + c4) = ov;
  }
}

extern "C" void kernel_launch(void* const* d_in, const int* in_sizes, int n_in,
                              void* d_out, int out_size, void* d_ws, size_t ws_size,
                              hipStream_t stream) {
  const __hip_bfloat16* x = (const __hip_bfloat16*)d_in[0];
  const __hip_bfloat16* w = (const __hip_bfloat16*)d_in[1];
  const __hip_bfloat16* bias = (const __hip_bfloat16*)d_in[2];
  const __hip_bfloat16* gamma = (const __hip_bfloat16*)d_in[3];
  const __hip_bfloat16* beta = (const __hip_bfloat16*)d_in[4];
  __hip_bfloat16* out = (__hip_bfloat16*)d_out;
  float* y = (float*)d_ws;
  float2* part = (float2*)((char*)d_ws + (size_t)2 * L_ * D_ * 4);
  hipFuncSetAttribute((const void*)conv_gemm_kernel, hipFuncAttributeMaxDynamicSharedMemorySize, LDS_BYTES);
  conv_gemm_kernel<<<256, 256, LDS_BYTES, stream>>>(x, w, bias, y, part);
  dim3 g2((L_ + 7) / 8, 2);
  norm_gelu_kernel<<<g2, 256, 0, stream>>>(y, part, gamma, beta, out);
}
```

```cpp
#include <hip/hip_runtime.h>
#include <hip/hip_bf16.h>

typedef __attribute__((ext_vector_type(8))) short bf16x8;
typedef __attribute__((ext_vector_type(4))) short bf16x4;
typedef __attribute__((ext_vector_type(16))) float f32x16;
typedef __attribute__((address_space(3))) bf16x4 lds_bf16x4;

constexpr int T_ = 4000, D_ = 256, KT = 31, L_ = 1985;
constexpr int BM = 128, BN = 32;
constexpr int MT = 16;
constexpr int XR = 144;
constexpr int XBLK = 2 * XR * 128;
constexpr int WST = 4096;
constexpr int RW = 8;
constexpr int PD = 7;
constexpr int NST = 4 * KT;
constexpr int LDS_BYTES = 2 * XBLK + RW * WST;
constexpr int NWB = 64;

__global__ __launch_bounds__(256, 1)
void conv_gemm_kernel(const __hip_bfloat16* __restrict__ x, const __hip_bfloat16* __restrict__ w,
                      const __hip_bfloat16* __restrict__ bias, float* __restrict__ y,
                      float2* __restrict__ part) {
  extern __shared__ __attribute__((aligned(16))) char sm[];
  const int tid = threadIdx.x, lane = tid & 63, wid = tid >> 6;
  const int bid = blockIdx.x;
  const int nt = bid & 7, rest = bid >> 3;
  const int b = rest >> 4, mt = rest & 15;
  const int t0 = 2 * mt * BM;
  const __hip_bfloat16* xb = x + (size_t)b * T_ * D_;

  auto load_x_piece = [&](int cb, int q) {
    int p = q / 18, rb = q % 18;
    int rl = rb * 8 + (lane >> 3);
    int ch = (lane & 7) ^ ((rl >> 1) & 7);
    int xrow = t0 + 2 * rl + p; xrow = xrow < T_ ? xrow : T_ - 1;
    __builtin_amdgcn_global_load_lds((const unsigned*)(xb + (size_t)xrow * D_ + cb * 64 + ch * 8),
        (unsigned*)(sm + (cb & 1) * XBLK + q * 1024), 16, 0, 0);
  };
  auto load_w = [&](int s) {
    int cb = s / KT, tap = s - cb * KT;
    int row = wid * 16 + (lane >> 2);
    __builtin_amdgcn_global_load_lds((const unsigned*)(w + ((size_t)(tap * 256 + cb * 64 + row)) * D_ + nt * BN + (lane & 3) * 8),
        (unsigned*)(sm + 2 * XBLK + (s % RW) * WST + wid * 1024), 16, 0, 0);
  };

  for (int q = wid; q < 36; q += 4) load_x_piece(0, q);
  #pragma unroll
  for (int s = 0; s < PD; ++s) load_w(s);

  f32x16 acc = {};
  const int r = lane & 31, h = lane >> 5;
  const int troff = (8 * h + ((lane & 15) >> 2)) * 64 + (((lane >> 4) & 1) * 16 + (lane & 3) * 4) * 2;
  const int arow0 = wid * 32 + r;

  for (int s = 0; s < NST; ++s) {
    const int cb = s / KT, tap = s - cb * KT;
    if (s + PD - 1 < NST) asm volatile("s_waitcnt vmcnt(6)" ::: "memory");
    else asm volatile("s_waitcnt vmcnt(0)" ::: "memory");
    __builtin_amdgcn_s_barrier();
    if (s + PD < NST) load_w(s + PD);
    if (tap < 9 && cb < 3) load_x_piece(cb + 1, wid + 4 * tap);
    const int j = tap >> 1, p = tap & 1;
    const int ar = arow0 + j;
    const char* abase = sm + (cb & 1) * XBLK + p * (XR * 128) + ar * 128;
    const int sw = (ar >> 1) & 7;
    const char* wbase = sm + 2 * XBLK + (s % RW) * WST + troff;
    bf16x8 a0, a1, a2, a3; bf16x4 p0, p1, p2, p3, p4, p5, p6, p7;
    const unsigned ab = (unsigned)(size_t)abase, wb = (unsigned)(size_t)wbase;
    asm volatile(
      "ds_read_b128 %0, %12\n\t"
      "ds_read_b64_tr_b16 %4, %16\n\t"
      "ds_read_b64_tr_b16 %5, %16 offset:256\n\t"
      "ds_read_b128 %1, %13\n\t"
      "ds_read_b64_tr_b16 %6, %16 offset:1024\n\t"
      "ds_read_b64_tr_b16 %7, %16 offset:1280\n\t"
      "ds_read_b128 %2, %14\n\t"
      "ds_read_b64_tr_b16 %8, %16 offset:2048\n\t"
      "ds_read_b64_tr_b16 %9, %16 offset:2304\n\t"
      "ds_read_b128 %3, %15\n\t"
      "ds_read_b64_tr_b16 %10, %16 offset:3072\n\t"
      "ds_read_b64_tr_b16 %11, %16 offset:3328\n\t"
      : "=&v"(a0), "=&v"(a1), "=&v"(a2), "=&v"(a3), "=&v"(p0), "=&v"(p1), "=&v"(p2), "=&v"(p3),
        "=&v"(p4), "=&v"(p5), "=&v"(p6), "=&v"(p7)
      : "v"(ab + (((0 + h) ^ sw) << 4)), "v"(ab + (((2 + h) ^ sw) << 4)),
        "v"(ab + (((4 + h) ^ sw) << 4)), "v"(ab + (((6 + h) ^ sw) << 4)), "v"(wb)
      : "memory");
    asm volatile("s_waitcnt lgkmcnt(9)" : "+v"(a0), "+v"(p0), "+v"(p1) :: "memory");
    acc = __builtin_amdgcn_mfma_f32_32x32x16_bf16(a0, __builtin_shufflevector(p0, p1, 0, 1, 2, 3, 4, 5, 6, 7), acc, 0, 0, 0);
    asm volatile("s_waitcnt lgkmcnt(6)" : "+v"(a1), "+v"(p2), "+v"(p3) :: "memory");
    acc = __builtin_amdgcn_mfma_f32_32x32x16_bf16(a1, __builtin_shufflevector(p2, p3, 0, 1, 2, 3, 4, 5, 6, 7), acc, 0, 0, 0);
    asm volatile("s_waitcnt lgkmcnt(3)" : "+v"(a2), "+v"(p4), "+v"(p5) :: "memory");
    acc = __builtin_amdgcn_mfma_f32_32x32x16_bf16(a2, __builtin_shufflevector(p4, p5, 0, 1, 2, 3, 4, 5, 6, 7), acc, 0, 0, 0);
    asm volatile("s_waitcnt lgkmcnt(0)" : "+v"(a3), "+v"(p6), "+v"(p7) :: "memory");
    acc = __builtin_amdgcn_mfma_f32_32x32x16_bf16(a3, __builtin_shufflevector(p6, p7, 0, 1, 2, 3, 4, 5, 6, 7), acc, 0, 0, 0);
  }

  const int col = nt * BN + r;
  const float bv = __bfloat162float(bias[col]);
  const int row0 = mt * BM + wid * 32;
  int cnt = L_ - row0; cnt = cnt > 32 ? 32 : cnt;
  float sum = 0.f;
  #pragma unroll
  for (int i = 0; i < 16; ++i) {
    int rr = (i & 3) + 8 * (i >> 2) + 4 * h;
    float v = acc[i] + bv;
    acc[i] = v;
    if (rr < cnt) { sum += v; y[((size_t)b * L_ + row0 + rr) * D_ + col] = v; }
  }
  sum += __shfl_xor(sum, 32);
  if (cnt > 0) {
    float mean = sum / (float)cnt;
    float m2 = 0.f;
    #pragma unroll
    for (int i = 0; i < 16; ++i) {
      int rr = (i & 3) + 8 * (i >> 2) + 4 * h;
      float d = acc[i] - mean;
      if (rr < cnt) m2 += d * d;
    }
    m2 += __shfl_xor(m2, 32);
    if (h == 0) part[((size_t)b * NWB + mt * 4 + wid) * D_ + col] = make_float2(mean, m2);
  }
}

__global__ __launch_bounds__(256)
void norm_gelu_kernel(const float* __restrict__ y, const float2* __restrict__ part,
                      const __hip_bfloat16* __restrict__ gamma, const __hip_bfloat16* __restrict__ beta,
                      __hip_bfloat16* __restrict__ out) {
  __shared__ float s_scale[256], s_shift[256];
  const int tid = threadIdx.x;
  const int b = blockIdx.y;
  {
    float n = 0.f, mean = 0.f, m2 = 0.f;
    for (int i = 0; i < 63; ++i) {
      float2 pm = part[((size_t)b * NWB + i) * D_ + tid];
      float c = (i < 62) ? 32.f : 1.f;
      float nn = n + c;
      float d = pm.x - mean;
      mean += d * (c / nn);
      m2 += pm.y + d * d * (n * c / nn);
      n = nn;
    }
    float var = m2 / n;
    float rs = rsqrtf(var + 1e-5f);
    float g = __bfloat162float(gamma[tid]), be = __bfloat162float(beta[tid]);
    s_scale[tid] = rs * g;
    s_shift[tid] = be - mean * rs * g;
  }
  __syncthreads();
  const int rows_per = 8;
  const int r0 = blockIdx.x * rows_per;
  const int c4 = (tid & 63) * 4;
  for (int rr = r0 + (tid >> 6); rr < r0 + rows_per && rr < L_; rr += 4) {
    float4 v = *(const float4*)(y + ((size_t)b * L_ + rr) * D_ + c4);
    float o[4] = {v.x, v.y, v.z, v.w};
    ushort4 ov;
    unsigned short* op = (unsigned short*)&ov;
    #pragma unroll
    for (int i = 0; i < 4; ++i) {
      float z = o[i] * s_scale[c4 + i] + s_shift[c4 + i];
      float u = 0.7978845608028654f * (z + 0.044715f * z * z * z);
      float t = tanhf(u);
      float g = 0.5f * z * (1.f + t);
      __hip_bfloat16 hb = __float2bfloat16(g);
      op[i] = *(unsigned short*)&hb;
    }
    *(ushort4*)(out + ((size_t)b * L_ + rr) * D_ + c4) = ov;
  }
}

extern "C" void kernel_launch(void* const* d_in, const int* in_sizes, int n_in,
                              void* d_out, int out_size, void* d_ws, size_t ws_size,
                              hipStream_t stream) {
  const __hip_bfloat16* x = (const __hip_bfloat16*)d_in[0];
  const __hip_bfloat16* w = (const __hip_bfloat16*)d_in[1];
  const __hip_bfloat16* bias = (const __hip_bfloat16*)d_in[2];
  const __hip_bfloat16* gamma = (const __hip_bfloat16*)d_in[3];
  const __hip_bfloat16* beta = (const __hip_bfloat16*)d_in[4];
  __hip_bfloat16* out = (__hip_bfloat16*)d_out;
  float* y = (float*)d_ws;
  float2* part = (float2*)((char*)d_ws + (size_t)2 * L_ * D_ * 4);
  hipFuncSetAttribute((const void*)conv_gemm_kernel, hipFuncAttributeMaxDynamicSharedMemorySize, LDS_BYTES);
  conv_gemm_kernel<<<256, 256, LDS_BYTES, stream>>>(x, w, bias, y, part);
  dim3 g2((L_ + 7) / 8, 2);
  norm_gelu_kernel<<<g2, 256, 0, stream>>>(y, part, gamma, beta, out);
}
```

```cpp
#include <hip/hip_runtime.h>
#include <hip/hip_bf16.h>

typedef __attribute__((ext_vector_type(8))) short bf16x8;
typedef __attribute__((ext_vector_type(4))) short bf16x4;
typedef __attribute__((ext_vector_type(16))) float f32x16;

constexpr int T_ = 4000, D_ = 256, KT = 31, L_ = 1985;
constexpr int BM = 128, BN = 32;
constexpr int XR = 144;
constexpr int XPAR = XR * 128;
constexpr int XBLK = 2 * XPAR;
constexpr int WST = 4096;
constexpr int RW = 8;
constexpr int NST = 4 * KT;
constexpr int LDS_BYTES = 2 * XBLK + RW * WST;
constexpr int NWB = 64;

struct Frags { bf16x8 a[4]; bf16x4 b[8]; };

template <int S>
__device__ __forceinline__ void read_frags(Frags& f, unsigned ab, unsigned wb) {
  constexpr int cb = S / KT, tap = S % KT, j = tap >> 1, p = tap & 1;
  constexpr int AO = p * XPAR + j * 16;
  constexpr int WO = (S % RW) * WST;
  asm volatile(
      "ds_read_b128 %0, %12 offset:%14\n\t"
      "ds_read_b64_tr_b16 %4, %13 offset:%18\n\t"
      "ds_read_b64_tr_b16 %5, %13 offset:%18+256\n\t"
      "ds_read_b128 %1, %12 offset:%15\n\t"
      "ds_read_b64_tr_b16 %6, %13 offset:%18+1024\n\t"
      "ds_read_b64_tr_b16 %7, %13 offset:%18+1280\n\t"
      "ds_read_b128 %2, %12 offset:%16\n\t"
      "ds_read_b64_tr_b16 %8, %13 offset:%18+2048\n\t"
      "ds_read_b64_tr_b16 %9, %13 offset:%18+2304\n\t"
      "ds_read_b128 %3, %12 offset:%17\n\t"
      "ds_read_b64_tr_b16 %10, %13 offset:%18+3072\n\t"
      "ds_read_b64_tr_b16 %11, %13 offset:%18+3328\n\t"
      : "=&v"(f.a[0]), "=&v"(f.a[1]), "=&v"(f.a[2]), "=&v"(f.a[3]), "=&v"(f.b[0]), "=&v"(f.b[1]), "=&v"(f.b[2]), "=&v"(f.b[3]),
        "=&v"(f.b[4]), "=&v"(f.b[5]), "=&v"(f.b[6]), "=&v"(f.b[7])
      : "v"(ab), "v"(wb), "i"(AO), "i"(AO + 2 * XR * 16), "i"(AO + 4 * XR * 16), "i"(AO + 6 * XR * 16), "i"(WO)
      : "memory");
}

struct Ctx {
  char* sm;
  const char* wsrc;
  const char* xsrc[9];
  unsigned ab, ab1, wb;
  int wid;
};

template <int S>
__device__ __forceinline__ void load_w(const Ctx& c) {
  constexpr int cb = S / KT, tap = S % KT;
  __builtin_amdgcn_global_load_lds((const unsigned*)(c.wsrc + (size_t)(tap * 256 + cb * 64) * 512),
      (unsigned*)(c.sm + 2 * XBLK + (S % RW) * WST + c.wid * 1024), 16, 0, 0);
}
template <int CB, int I>
__device__ __forceinline__ void load_x(const Ctx& c) {
  __builtin_amdgcn_global_load_lds((const unsigned*)(c.xsrc[I] + CB * 128),
      (unsigned*)(c.sm + (CB & 1) * XBLK + (c.wid + 4 * I) * 1024), 16, 0, 0);
}

#define MFMA(A, B0, B1) acc = __builtin_amdgcn_mfma_f32_32x32x16_bf16(A, __builtin_shufflevector(B0, B1, 0, 1, 2, 3, 4, 5, 6, 7), acc, 0, 0, 0)

template <int S>
__device__ __forceinline__ void run_stage(const Ctx& c, Frags& cur, Frags& nxt, f32x16& acc) {
  constexpr int cb = S / KT, tap = S % KT;
  if constexpr (S + 1 < NST) {
    if constexpr (S + 6 < NST) asm volatile("s_waitcnt vmcnt(5)" ::: "memory");
    else asm volatile("s_waitcnt vmcnt(0)" ::: "memory");
    __builtin_amdgcn_s_barrier();
    if constexpr (S + 7 < NST) load_w<S + 7>(c);
    if constexpr (tap < 9 && cb < 3) load_x<cb + 1, tap>(c);
    read_frags<S + 1>(nxt, (((S + 1) / KT) & 1) ? c.ab1 : c.ab, c.wb);
    asm volatile("s_waitcnt lgkmcnt(12)" : "+v"(cur.a[0]), "+v"(cur.a[1]), "+v"(cur.a[2]), "+v"(cur.a[3]),
                 "+v"(cur.b[0]), "+v"(cur.b[1]), "+v"(cur.b[2]), "+v"(cur.b[3]), "+v"(cur.b[4]), "+v"(cur.b[5]), "+v"(cur.b[6]), "+v"(cur.b[7]) :: "memory");
  } else {
    asm volatile("s_waitcnt lgkmcnt(0)" : "+v"(cur.a[0]), "+v"(cur.a[1]), "+v"(cur.a[2]), "+v"(cur.a[3]),
                 "+v"(cur.b[0]), "+v"(cur.b[1]), "+v"(cur.b[2]), "+v"(cur.b[3]), "+v"(cur.b[4]), "+v"(cur.b[5]), "+v"(cur.b[6]), "+v"(cur.b[7]) :: "memory");
  }
  MFMA(cur.a[0], cur.b[0], cur.b[1]);
  MFMA(cur.a[1], cur.b[2], cur.b[3]);
  MFMA(cur.a[2], cur.b[4], cur.b[5]);
  MFMA(cur.a[3], cur.b[6], cur.b[7]);
  __builtin_amdgcn_sched_barrier(0);
  if constexpr (S + 1 < NST) run_stage<S + 1>(c, nxt, cur, acc);
}

__global__ __launch_bounds__(256, 1)
void conv_gemm_kernel(const __hip_bfloat16* __restrict__ x, const __hip_bfloat16* __restrict__ w,
                      const __hip_bfloat16* __restrict__ bias, float* __restrict__ y,
                      float2* __restrict__ part) {
  extern __shared__ __attribute__((aligned(16))) char sm[];
  const int tid = threadIdx.x, lane = tid & 63;
  const int wid = __builtin_amdgcn_readfirstlane(tid >> 6);
  const int bid = blockIdx.x;
  const int nt = bid & 7, rest = bid >> 3;
  const int b = rest >> 4, mt = rest & 15;
  const int t0 = 2 * mt * BM;
  const char* xb = (const char*)(x + (size_t)b * T_ * D_);
  const int r = lane & 31, h = lane >> 5;

  Ctx c;
  c.sm = sm; c.wid = wid;
  c.wsrc = (const char*)(w + (size_t)(wid * 16 + (lane >> 2)) * D_ + nt * BN + (lane & 3) * 8);
  #pragma unroll
  for (int i = 0; i < 9; ++i) {
    int q = wid + 4 * i;
    int e = q * 64 + lane;
    int p = e / (8 * XR); int rem = e - p * (8 * XR);
    int ch = rem / XR, rl = rem - ch * XR;
    int xrow = t0 + 2 * rl + p; xrow = xrow < T_ ? xrow : T_ - 1;
    c.xsrc[i] = xb + (size_t)xrow * 512 + ch * 16;
  }
  const unsigned smb = (unsigned)(size_t)sm;
  c.ab = smb + h * (XR * 16) + (wid * 32 + r) * 16;
  c.ab1 = c.ab + XBLK;
  c.wb = smb + 2 * XBLK + (8 * h + ((lane & 15) >> 2)) * 64 + (((lane >> 4) & 1) * 16 + (lane & 3) * 4) * 2;

  load_x<0, 0>(c); load_x<0, 1>(c); load_x<0, 2>(c); load_x<0, 3>(c); load_x<0, 4>(c);
  load_x<0, 5>(c); load_x<0, 6>(c); load_x<0, 7>(c); load_x<0, 8>(c);
  load_w<0>(c); load_w<1>(c); load_w<2>(c); load_w<3>(c); load_w<4>(c); load_w<5>(c); load_w<6>(c);
  asm volatile("s_waitcnt vmcnt(6)" ::: "memory");
  __builtin_amdgcn_s_barrier();
  f32x16 acc = {};
  Frags f0, f1;
  read_frags<0>(f0, c.ab, c.wb);
  run_stage<0>(c, f0, f1, acc);

  const int col = nt * BN + r;
  const float bv = __bfloat162float(bias[col]);
  const int row0 = mt * BM + wid * 32;
  int cnt = L_ - row0; cnt = cnt > 32 ? 32 : cnt;
  if (cnt <= 0) return;
  float* yp = y + ((size_t)b * L_ + row0 + 4 * h) * D_ + col;
  float sum = 0.f;
  if (cnt == 32) {
    #pragma unroll
    for (int i = 0; i < 16; ++i) {
      float v = acc[i] + bv; acc[i] = v; sum += v;
      yp[((i & 3) + 8 * (i >> 2)) * D_] = v;
    }
  } else {
    #pragma unroll
    for (int i = 0; i < 16; ++i) {
      int rr = (i & 3) + 8 * (i >> 2) + 4 * h;
      float v = acc[i] + bv; acc[i] = v;
      if (rr < cnt) { sum += v; yp[((i & 3) + 8 * (i >> 2)) * D_] = v; }
    }
  }
  sum += __shfl_xor(sum, 32);
  float mean = sum / (float)cnt;
  float m2 = 0.f;
  #pragma unroll
  for (int i = 0; i < 16; ++i) {
    int rr = (i & 3) + 8 * (i >> 2) + 4 * h;
    float d = acc[i] - mean;
    m2 += (rr < cnt) ? d * d : 0.f;
  }
  m2 += __shfl_xor(m2, 32);
  if (h == 0) part[((size_t)b * NWB + mt * 4 + wid) * D_ + col] = make_float2(mean, m2);
}

__global__ __launch_bounds__(256)
void norm_gelu_kernel(const float* __restrict__ y, const float2* __restrict__ part,
                      const __hip_bfloat16* __restrict__ gamma, const __hip_bfloat16* __restrict__ beta,
                      __hip_bfloat16* __restrict__ out) {
  __shared__ float s_scale[256], s_shift[256];
  const int tid = threadIdx.x;
  const int b = blockIdx.y;
  {
    float2 pm[63];
    #pragma unroll
    for (int i = 0; i < 63; ++i) pm[i] = part[((size_t)b * NWB + i) * D_ + tid];
    float s = 0.f;
    #pragma unroll
    for (int i = 0; i < 62; ++i) s += pm[i].x;
    float mean = (s * 32.f + pm[62].x) * (1.f / (float)L_);
    float m2 = 0.f;
    #pragma unroll
    for (int i = 0; i < 62; ++i) { float d = pm[i].x - mean; m2 += pm[i].y + 32.f * d * d; }
    { float d = pm[62].x - mean; m2 += pm[62].y + d * d; }
    float var = m2 * (1.f / (float)L_);
    float rs = rsqrtf(var + 1e-5f);
    float g = __bfloat162float(gamma[tid]), be = __bfloat162float(beta[tid]);
    s_scale[tid] = rs * g;
    s_shift[tid] = be - mean * rs * g;
  }
  __syncthreads();
  const int rows_per = 8;
  const int r0 = blockIdx.x * rows_per;
  const int c4 = (tid & 63) * 4;
  for (int rr = r0 + (tid >> 6); rr < r0 + rows_per && rr < L_; rr += 4) {
    float4 v = *(const float4*)(y + ((size_t)b * L_ + rr) * D_ + c4);
    float o[4] = {v.x, v.y, v.z, v.w};
    ushort4 ov;
    unsigned short* op = (unsigned short*)&ov;
    #pragma unroll
    for (int i = 0; i < 4; ++i) {
      float z = o[i] * s_scale[c4 + i] + s_shift[c4 + i];
      float u = 0.7978845608028654f * (z + 0.044715f * z * z * z);
      float t = tanhf(u);
      float g = 0.5f * z * (1.f + t);
      __hip_bfloat16 hb = __float2bfloat16(g);
      op[i] = *(unsigned short*)&hb;
    }
    *(ushort4*)(out + ((size_t)b * L_ + rr) * D_ + c4) = ov;
  }
}

extern "C" void kernel_launch(void* const* d_in, const int* in_sizes, int n_in,
                              void* d_out, int out_size, void* d_ws, size_t ws_size,
                              hipStream_t stream) {
  const __hip_bfloat16* x = (const __hip_bfloat16*)d_in[0];
  const __hip_bfloat16* w = (const __hip_bfloat16*)d_in[1];
  const __hip_bfloat16* bias = (const __hip_bfloat16*)d_in[2];
  const __hip_bfloat16* gamma = (const __hip_bfloat16*)d_in[3];
  const __hip_bfloat16* beta = (const __hip_bfloat16*)d_in[4];
  __hip_bfloat16* out = (__hip_bfloat16*)d_out;
  float* y = (float*)d_ws;
  float2* part = (float2*)((char*)d_ws + (size_t)2 * L_ * D_ * 4);
  (void)hipFuncSetAttribute((const void*)conv_gemm_kernel, hipFuncAttributeMaxDynamicSharedMemorySize, LDS_BYTES);
  conv_gemm_kernel<<<256, 256, LDS_BYTES, stream>>>(x, w, bias, y, part);
  dim3 g2((L_ + 7) / 8, 2);
  norm_gelu_kernel<<<g2, 256, 0, stream>>>(y, part, gamma, beta, out);
}
```

```cpp
#include <hip/hip_runtime.h>
#include <hip/hip_bf16.h>

typedef __attribute__((ext_vector_type(8))) short bf16x8;
typedef __attribute__((ext_vector_type(4))) short bf16x4;
typedef __attribute__((ext_vector_type(16))) float f32x16;

constexpr int T_ = 4000, D_ = 256, KT = 31, L_ = 1985;
constexpr int BM = 128, BN = 32;
constexpr int MT = 16;
constexpr int XR = 144;
constexpr int XPAR = XR * 128;
constexpr int XBLK = 2 * XPAR;
constexpr int WST = 8192;
constexpr int RW = 8;
constexpr int LDS_BYTES = 2 * XBLK + RW * WST;

struct Frags { bf16x8 a[4]; bf16x4 b[8]; };

template <int J, int KC>
__device__ __forceinline__ void read_grp(Frags& f, unsigned ab, unsigned wb) {
  constexpr int AO = J * 16 + KC * 2 * XR * 16;
  constexpr int WO = (J % RW) * WST + KC * 1024;
  asm volatile(
      "ds_read_b128 %0, %3 offset:%5\n\t"
      "ds_read_b64_tr_b16 %1, %4 offset:%6\n\t"
      "ds_read_b64_tr_b16 %2, %4 offset:%6+256\n\t"
      : "=&v"(f.a[KC]), "=&v"(f.b[2 * KC]), "=&v"(f.b[2 * KC + 1])
      : "v"(ab), "v"(wb), "i"(AO), "i"(WO)
      : "memory");
}
template <int J>
__device__ __forceinline__ void read_frags(Frags& f, unsigned ab, unsigned wb) {
  read_grp<J, 0>(f, ab, wb); read_grp<J, 1>(f, ab, wb); read_grp<J, 2>(f, ab, wb); read_grp<J, 3>(f, ab, wb);
}

struct Ctx {
  char* sm;
  const char* wsrc;
  const char* xsrc[5];
  unsigned ab, abn, wb;
  int wid, grp;
  int xdst;
  bool more;
};

template <int V>
__device__ __forceinline__ void load_w(const Ctx& c) {
  constexpr int nb = V / 16, j = V % 16;
  if (j == 15 && c.grp) return;
  __builtin_amdgcn_global_load_lds((const unsigned*)(c.wsrc + (size_t)(2 * j * 256 + nb * 64) * 512),
      (unsigned*)(c.sm + 2 * XBLK + (j % RW) * WST + c.wid * 1024), 16, 0, 0);
}
template <int I>
__device__ __forceinline__ void load_x(const Ctx& c) {
  if (I == 4 && c.grp) return;
  __builtin_amdgcn_global_load_lds((const unsigned*)(c.xsrc[I]),
      (unsigned*)(c.sm + c.xdst + (c.wid + 8 * I) * 1024), 16, 0, 0);
}

#define MFMA(A, B0, B1) acc = __builtin_amdgcn_mfma_f32_32x32x16_bf16(A, __builtin_shufflevector(B0, B1, 0, 1, 2, 3, 4, 5, 6, 7), acc, 0, 0, 0)
#define LGKM(N, F) asm volatile("s_waitcnt lgkmcnt(" #N ")" : "+v"(F.a[0]), "+v"(F.a[1]), "+v"(F.a[2]), "+v"(F.a[3]), \
                 "+v"(F.b[0]), "+v"(F.b[1]), "+v"(F.b[2]), "+v"(F.b[3]), "+v"(F.b[4]), "+v"(F.b[5]), "+v"(F.b[6]), "+v"(F.b[7]) :: "memory")
#define STEP(KC) \
    asm volatile("s_waitcnt lgkmcnt(9)" : "+v"(cur.a[KC]), "+v"(cur.b[2 * KC]), "+v"(cur.b[2 * KC + 1]) :: "memory"); \
    MFMA(cur.a[KC], cur.b[2 * KC], cur.b[2 * KC + 1]); \
    __builtin_amdgcn_sched_barrier(0); \
    read_grp<RT, KC>(nxt, rab, c.wb);
#define MFMA4 \
    MFMA(cur.a[0], cur.b[0], cur.b[1]); MFMA(cur.a[1], cur.b[2], cur.b[3]); \
    MFMA(cur.a[2], cur.b[4], cur.b[5]); MFMA(cur.a[3], cur.b[6], cur.b[7]);

template <int J>
__device__ __forceinline__ void run_stage(const Ctx& c, Frags& cur, Frags& nxt, f32x16& acc) {
  const bool last = (J == 15) && !c.more;
  if (!last) {
    if constexpr (J < 9) asm volatile("s_waitcnt vmcnt(4)" ::: "memory");
    else { if (c.more) asm volatile("s_waitcnt vmcnt(4)" ::: "memory"); else asm volatile("s_waitcnt vmcnt(0)" ::: "memory"); }
    __builtin_amdgcn_s_barrier();
    if constexpr (J + 7 < 16) load_w<J + 7>(c);
    else { if (c.more) load_w<J + 7>(c); }
    if constexpr (J < 5) { if (c.more) load_x<J>(c); }
    constexpr int RT = (J + 1) % 16;
    const unsigned rab = (J == 15) ? c.abn : c.ab;
    if constexpr (J < 14) {
      STEP(0) STEP(1) STEP(2) STEP(3)
    } else if constexpr (J == 14) {
      if (c.grp) { LGKM(0, cur); MFMA4 }
      else { STEP(0) STEP(1) STEP(2) STEP(3) }
    } else {
      if (c.grp) { read_frags<RT>(nxt, rab, c.wb); }
      else { STEP(0) STEP(1) STEP(2) STEP(3) }
    }
  } else {
    if (!c.grp) { LGKM(0, cur); MFMA4 }
  }
  __builtin_amdgcn_sched_barrier(0);
  if constexpr (J < 15) run_stage<J + 1>(c, nxt, cur, acc);
}

__global__ __launch_bounds__(512, 1)
void conv_gemm_kernel(const __hip_bfloat16* __restrict__ x, const __hip_bfloat16* __restrict__ w,
                      const __hip_bfloat16* __restrict__ bias, float* __restrict__ y,
                      float2* __restrict__ part) {
  extern __shared__ __attribute__((aligned(16))) char sm[];
  const int tid = threadIdx.x, lane = tid & 63;
  const int wid = __builtin_amdgcn_readfirstlane(tid >> 6);
  const int grp = wid >> 2, wq = wid & 3;
  const int bid = blockIdx.x;
  const int nt = bid & 7, rest = bid >> 3;
  const int b = rest >> 4, mt = rest & 15;
  const int t0 = 2 * mt * BM;
  const char* xb = (const char*)(x + (size_t)b * T_ * D_);
  const int r = lane & 31, h = lane >> 5;

  Ctx c;
  c.sm = sm; c.wid = wid; c.grp = grp;
  c.wsrc = (const char*)(w + (size_t)(grp * 256 + wq * 16 + (lane >> 2)) * D_ + nt * BN + (lane & 3) * 8);
  #pragma unroll
  for (int i = 0; i < 5; ++i) {
    int q = wid + 8 * i; q = q < 36 ? q : 35;
    int e = q * 64 + lane;
    int p = e / (8 * XR); int rem = e - p * (8 * XR);
    int ch = rem / XR, rl = rem - ch * XR;
    int xrow = t0 + 2 * rl + p; xrow = xrow < T_ ? xrow : T_ - 1;
    c.xsrc[i] = xb + (size_t)xrow * 512 + ch * 16;
  }
  const unsigned smb = (unsigned)(size_t)sm;
  c.ab = smb + grp * XPAR + h * (XR * 16) + (wq * 32 + r) * 16;
  c.abn = c.ab + XBLK;
  c.wb = smb + 2 * XBLK + grp * 4096 + (8 * h + ((lane & 15) >> 2)) * 64 + (((lane >> 4) & 1) * 16 + (lane & 3) * 4) * 2;

  c.xdst = 0;
  load_x<0>(c); load_x<1>(c); load_x<2>(c); load_x<3>(c); load_x<4>(c);
  load_w<0>(c); load_w<1>(c); load_w<2>(c); load_w<3>(c); load_w<4>(c); load_w<5>(c); load_w<6>(c);
  asm volatile("s_waitcnt vmcnt(6)" ::: "memory");
  __builtin_amdgcn_s_barrier();
  f32x16 acc = {};
  Frags f0, f1;
  read_frags<0>(f0, c.ab, c.wb);
  #pragma unroll 1
  for (int cb = 0; cb < 4; ++cb) {
    c.more = cb < 3;
    c.xdst = ((cb + 1) & 1) * XBLK;
    #pragma unroll
    for (int i = 0; i < 5; ++i) c.xsrc[i] += 128;
    run_stage<0>(c, f0, f1, acc);
    unsigned u = c.ab; c.ab = c.abn; c.abn = u;
    c.wsrc += 64 * 512;
  }

  float* red = (float*)sm;
  __syncthreads();
  if (grp) {
    #pragma unroll
    for (int i = 0; i < 16; ++i) red[(wq * 16 + i) * 64 + lane] = acc[i];
  }
  __syncthreads();
  const int col = nt * BN + r;
  const int row0 = mt * BM + wq * 32;
  int cnt = L_ - row0; cnt = cnt > 32 ? 32 : (cnt < 0 ? 0 : cnt);
  int cnt_wg = L_ - mt * BM; cnt_wg = cnt_wg > BM ? BM : cnt_wg;
  float* st = (float*)(sm + 32768);
  if (!grp) {
    const float bv = __bfloat162float(bias[col]);
    float* yp = y + ((size_t)b * L_ + row0 + 4 * h) * D_ + col;
    float sum = 0.f;
    #pragma unroll
    for (int i = 0; i < 16; ++i) {
      int rr = (i & 3) + 8 * (i >> 2) + 4 * h;
      float v = acc[i] + red[(wq * 16 + i) * 64 + lane] + bv; acc[i] = v;
      if (rr < cnt) { sum += v; yp[((i & 3) + 8 * (i >> 2)) * D_] = v; }
    }
    sum += __shfl_xor(sum, 32);
    if (h == 0) st[wq * 32 + r] = sum;
  }
  __syncthreads();
  float mean = 0.f;
  if (!grp) {
    mean = (st[r] + st[32 + r] + st[64 + r] + st[96 + r]) / (float)cnt_wg;
    float m2 = 0.f;
    #pragma unroll
    for (int i = 0; i < 16; ++i) {
      int rr = (i & 3) + 8 * (i >> 2) + 4 * h;
      float d = acc[i] - mean;
      m2 += (rr < cnt) ? d * d : 0.f;
    }
    m2 += __shfl_xor(m2, 32);
    if (h == 0) st[128 + wq * 32 + r] = m2;
  }
  __syncthreads();
  if (tid < 32)
    part[((size_t)b * MT + mt) * D_ + col] = make_float2(mean, st[128 + r] + st[160 + r] + st[192 + r] + st[224 + r]);
}

__global__ __launch_bounds__(256)
void norm_gelu_kernel(const float* __restrict__ y, const float2* __restrict__ part,
                      const __hip_bfloat16* __restrict__ gamma, const __hip_bfloat16* __restrict__ beta,
                      __hip_bfloat16* __restrict__ out) {
  __shared__ float s_scale[256], s_shift[256];
  const int tid = threadIdx.x;
  const int b = blockIdx.y;
  const int rows_per = 8;
  const int r0 = blockIdx.x * rows_per;
  const int c4 = (tid & 63) * 4;
  float4 v[2];
  #pragma unroll
  for (int k = 0; k < 2; ++k) {
    int rr = r0 + (tid >> 6) + 4 * k; rr = rr < L_ ? rr : L_ - 1;
    v[k] = *(const float4*)(y + ((size_t)b * L_ + rr) * D_ + c4);
  }
  {
    float2 pm[16];
    #pragma unroll
    for (int i = 0; i < 16; ++i) pm[i] = part[((size_t)b * MT + i) * D_ + tid];
    float s = 0.f;
    #pragma unroll
    for (int i = 0; i < 15; ++i) s += pm[i].x;
    const float nl = (float)(L_ - 15 * BM);
    float mean = (s * (float)BM + pm[15].x * nl) * (1.f / (float)L_);
    float m2 = 0.f;
    #pragma unroll
    for (int i = 0; i < 15; ++i) { float d = pm[i].x - mean; m2 += pm[i].y + (float)BM * d * d; }
    { float d = pm[15].x - mean; m2 += pm[15].y + nl * d * d; }
    float var = m2 * (1.f / (float)L_);
    float rs = rsqrtf(var + 1e-5f);
    float g = __bfloat162float(gamma[tid]), be = __bfloat162float(beta[tid]);
    s_scale[tid] = rs * g;
    s_shift[tid] = be - mean * rs * g;
  }
  __syncthreads();
  #pragma unroll
  for (int k = 0; k < 2; ++k) {
    int rr = r0 + (tid >> 6) + 4 * k;
    if (rr >= L_) break;
    float o[4] = {v[k].x, v[k].y, v[k].z, v[k].w};
    ushort4 ov;
    unsigned short* op = (unsigned short*)&ov;
    #pragma unroll
    for (int i = 0; i < 4; ++i) {
      float z = o[i] * s_scale[c4 + i] + s_shift[c4 + i];
      float u = 0.7978845608028654f * (z + 0.044715f * z * z * z);
      float t = tanhf(u);
      float g = 0.5f * z * (1.f + t);
      __hip_bfloat16 hb = __float2bfloat16(g);
      op[i] = *(unsigned short*)&hb;
    }
    *(ushort4*)(out + ((size_t)b * L_ + rr) * D_ + c4) = ov;
  }
}

extern "C" void kernel_launch(void* const* d_in, const int* in_sizes, int n_in,
                              void* d_out, int out_size, void* d_ws, size_t ws_size,
                              hipStream_t stream) {
  const __hip_bfloat16* x = (const __hip_bfloat16*)d_in[0];
  const __hip_bfloat16* w = (const __hip_bfloat16*)d_in[1];
  const __hip_bfloat16* bias = (const __hip_bfloat16*)d_in[2];
  const __hip_bfloat16* gamma = (const __hip_bfloat16*)d_in[3];
  const __hip_bfloat16* beta = (const __hip_bfloat16*)d_in[4];
  __hip_bfloat16* out = (__hip_bfloat16*)d_out;
  float* y = (float*)d_ws;
  float2* part = (float2*)((char*)d_ws + (size_t)2 * L_ * D_ * 4);
  (void)hipFuncSetAttribute((const void*)conv_gemm_kernel, hipFuncAttributeMaxDynamicSharedMemorySize, LDS_BYTES);
  conv_gemm_kernel<<<256, 512, LDS_BYTES, stream>>>(x, w, bias, y, part);
  dim3 g2((L_ + 7) / 8, 2);
  norm_gelu_kernel<<<g2, 256, 0, stream>>>(y, part, gamma, beta, out);
}
```

```cpp
#include <hip/hip_runtime.h>
#include <hip/hip_bf16.h>

typedef __attribute__((ext_vector_type(8))) short bf16x8;
typedef __attribute__((ext_vector_type(4))) short bf16x4;
typedef __attribute__((ext_vector_type(16))) float f32x16;

constexpr int T_ = 4000, D_ = 256, L_ = 1985;
constexpr int BM = 256, BN = 64;
constexpr int XR = 272;
constexpr int XBLK = XR * 128;
constexpr int WST = 8192;
constexpr int RW = 8;
constexpr int WOFF = 2 * XBLK;
constexpr int LDS_BYTES = WOFF + RW * WST;
constexpr int RT2 = 64;
constexpr int NT2 = 32;

struct Frags { bf16x8 a[2]; bf16x4 b[4]; };

template <int CB, int J, int KC>
__device__ __forceinline__ void read_frags(Frags& f, unsigned ab, unsigned wb0, unsigned wb1) {
  constexpr int AO = CB * XBLK + J * 16 + KC * 2 * XR * 16;
  constexpr int WO = (J % RW) * WST + KC * 2048;
  asm volatile(
      "ds_read_b128 %0, %6 offset:%9\n\t"
      "ds_read_b64_tr_b16 %2, %7 offset:%10\n\t"
      "ds_read_b64_tr_b16 %3, %7 offset:%10+512\n\t"
      "ds_read_b128 %1, %6 offset:%9+512\n\t"
      "ds_read_b64_tr_b16 %4, %8 offset:%10\n\t"
      "ds_read_b64_tr_b16 %5, %8 offset:%10+512\n\t"
      : "=&v"(f.a[0]), "=&v"(f.a[1]), "=&v"(f.b[0]), "=&v"(f.b[1]), "=&v"(f.b[2]), "=&v"(f.b[3])
      : "v"(ab), "v"(wb0), "v"(wb1), "i"(AO), "i"(WO)
      : "memory");
}

struct Ctx {
  char* sm;
  const char* wsrc;
  unsigned ab, wb0, wb1;
  int wid, p;
};

template <int V>
__device__ __forceinline__ void load_w(const Ctx& c) {
  constexpr int cb = V / 16, j = V % 16;
  if (V >= 32) return;
  if (j == 15 && c.p) return;
  const char* s = c.wsrc + (size_t)(2 * j * 256 + cb * 64) * 512;
  __builtin_amdgcn_global_load_lds((const unsigned*)s, (unsigned*)(c.sm + WOFF + (j % RW) * WST + c.wid * 2048), 16, 0, 0);
  __builtin_amdgcn_global_load_lds((const unsigned*)(s + 8 * 512), (unsigned*)(c.sm + WOFF + (j % RW) * WST + c.wid * 2048 + 1024), 16, 0, 0);
}

#define B8(F, N) __builtin_shufflevector(F.b[2 * N], F.b[2 * N + 1], 0, 1, 2, 3, 4, 5, 6, 7)
#define MFMA(M, N, F) acc[M][N] = __builtin_amdgcn_mfma_f32_32x32x16_bf16(F.a[M], B8(F, N), acc[M][N], 0, 0, 0)
#define LGKM(NN, F) asm volatile("s_waitcnt lgkmcnt(" #NN ")" : "+v"(F.a[0]), "+v"(F.a[1]), "+v"(F.b[0]), "+v"(F.b[1]), "+v"(F.b[2]), "+v"(F.b[3]) :: "memory")
#define MFMA4(F) MFMA(0, 0, F); MFMA(1, 0, F); MFMA(0, 1, F); MFMA(1, 1, F);

template <int V>
__device__ __forceinline__ void run_stage(const Ctx& c, Frags& f0, Frags& f1, f32x16 (&acc)[2][2]) {
  constexpr int CB = V / 16, J = V % 16;
  const bool skip = (J == 15) && c.p;
  if (!skip) {
    const bool lastp = (V == 31) || (V == 30 && c.p);
    if (!lastp) {
      if constexpr (V < 24) asm volatile("s_waitcnt vmcnt(8)" ::: "memory");
      else asm volatile("s_waitcnt vmcnt(0)" ::: "memory");
      __builtin_amdgcn_s_barrier();
      if constexpr (V == 16) { if (c.p) load_w<22>(c); }
      load_w<V + 7>(c);
    }
    LGKM(0, f0); MFMA(0, 0, f0); __builtin_amdgcn_sched_barrier(0);
    read_frags<CB, J, 1>(f1, c.ab, c.wb0, c.wb1);
    MFMA(1, 0, f0); MFMA(0, 1, f0); MFMA(1, 1, f0); __builtin_amdgcn_sched_barrier(0);
    LGKM(0, f1); MFMA(0, 0, f1); __builtin_amdgcn_sched_barrier(0);
    read_frags<CB, J, 2>(f0, c.ab, c.wb0, c.wb1);
    MFMA(1, 0, f1); MFMA(0, 1, f1); MFMA(1, 1, f1); __builtin_amdgcn_sched_barrier(0);
    LGKM(0, f0); MFMA(0, 0, f0); __builtin_amdgcn_sched_barrier(0);
    read_frags<CB, J, 3>(f1, c.ab, c.wb0, c.wb1);
    MFMA(1, 0, f0); MFMA(0, 1, f0); MFMA(1, 1, f0); __builtin_amdgcn_sched_barrier(0);
    LGKM(0, f1); MFMA(0, 0, f1); __builtin_amdgcn_sched_barrier(0);
    if (!lastp) {
      if constexpr (J == 14) {
        if (c.p) read_frags<(CB + 1) & 1, 0, 0>(f0, c.ab, c.wb0, c.wb1);
        else read_frags<CB, 15, 0>(f0, c.ab, c.wb0, c.wb1);
      } else if constexpr (J == 15) read_frags<(CB + 1) & 1, 0, 0>(f0, c.ab, c.wb0, c.wb1);
      else read_frags<CB, J + 1, 0>(f0, c.ab, c.wb0, c.wb1);
    }
    MFMA(1, 0, f1); MFMA(0, 1, f1); MFMA(1, 1, f1); __builtin_amdgcn_sched_barrier(0);
  }
  if constexpr (V < 31) run_stage<V + 1>(c, f0, f1, acc);
}

__global__ __launch_bounds__(256, 1)
void conv_gemm_kernel(const __hip_bfloat16* __restrict__ x, const __hip_bfloat16* __restrict__ w,
                      float* __restrict__ yp) {
  extern __shared__ __attribute__((aligned(16))) char sm[];
  const int tid = threadIdx.x, lane = tid & 63;
  const int wid = __builtin_amdgcn_readfirstlane(tid >> 6);
  const int bid = blockIdx.x;
  const int nt = (bid & 7) >> 1, p = bid & 1, rest = bid >> 3;
  const int chh = rest & 1, bm = rest >> 1;
  const int b = bm >> 3, mt = bm & 7;
  const int t0 = 2 * mt * BM;
  const char* xb = (const char*)(x + (size_t)b * T_ * D_) + chh * 256;
  const int r = lane & 31, h = lane >> 5;

  Ctx c;
  c.sm = sm; c.wid = wid; c.p = p;
  {
    int row = wid * 16 + (lane >> 3);
    int pc = lane & 7;
    int lch = (((pc >> 2) ^ ((row >> 1) & 1)) << 2) | (pc & 3);
    c.wsrc = (const char*)(w + (size_t)(p * 256 + chh * 128 + row) * D_ + nt * BN + lch * 8);
  }
  #pragma unroll
  for (int i = 0; i < 17; ++i) {
    int q = wid + 4 * i;
    int e = q * 64 + lane;
    int cc = e / XR, rl = e - cc * XR;
    int xrow = t0 + 2 * rl + p; xrow = xrow < T_ ? xrow : T_ - 1;
    __builtin_amdgcn_global_load_lds((const unsigned*)(xb + (size_t)xrow * 512 + cc * 16), (unsigned*)(sm + q * 1024), 16, 0, 0);
  }
  const unsigned smb = (unsigned)(size_t)sm;
  c.ab = smb + h * (XR * 16) + (wid * 64 + r) * 16;
  {
    int q = (lane & 15) >> 2;
    unsigned base = smb + WOFF + (8 * h + q) * 128 + ((lane >> 4) & 1) * 32 + (lane & 3) * 8;
    c.wb0 = base + (((q >> 1) & 1) << 6);
    c.wb1 = base + ((((q >> 1) & 1) ^ 1) << 6);
  }
  load_w<0>(c); load_w<1>(c); load_w<2>(c); load_w<3>(c); load_w<4>(c); load_w<5>(c); load_w<6>(c);
  asm volatile("s_waitcnt vmcnt(12)" ::: "memory");
  __builtin_amdgcn_s_barrier();
  f32x16 acc[2][2] = {};
  Frags f0, f1;
  read_frags<0, 0, 0>(f0, c.ab, c.wb0, c.wb1);
  run_stage<0>(c, f0, f1, acc);

  const int split = chh * 2 + p;
  float* o = yp + ((size_t)split * 2 + b) * ((size_t)L_ * D_);
  #pragma unroll
  for (int m = 0; m < 2; ++m) {
    const int row0 = mt * BM + wid * 64 + m * 32 + 4 * h;
    #pragma unroll
    for (int n = 0; n < 2; ++n) {
      const int col = nt * BN + n * 32 + r;
      #pragma unroll
      for (int i = 0; i < 16; ++i) {
        int row = row0 + (i & 3) + 8 * (i >> 2);
        if (row < L_) o[(size_t)row * D_ + col] = acc[m][n][i];
      }
    }
  }
}

__global__ __launch_bounds__(256)
void reduce_stats_kernel(const float* __restrict__ yp, const __hip_bfloat16* __restrict__ bias,
                         float* __restrict__ y, float2* __restrict__ part) {
  __shared__ float4 s_red[16][16];
  const int tid = threadIdx.x;
  const int ct = blockIdx.x & 3, rt = blockIdx.x >> 2, b = blockIdx.y;
  const int cq = tid & 15, rq = tid >> 4;
  const int col = ct * 64 + cq * 4;
  const size_t plane = (size_t)L_ * D_;
  float4 v[4];
  int cnt = L_ - rt * RT2; cnt = cnt > RT2 ? RT2 : cnt;
  float4 bv = make_float4(__bfloat162float(bias[col]), __bfloat162float(bias[col + 1]), __bfloat162float(bias[col + 2]), __bfloat162float(bias[col + 3]));
  float4 t[4][4];
  #pragma unroll
  for (int k = 0; k < 4; ++k) {
    int row = rt * RT2 + rq + 16 * k; row = row < L_ ? row : L_ - 1;
    #pragma unroll
    for (int s = 0; s < 4; ++s) t[k][s] = *(const float4*)(yp + ((size_t)s * 2 + b) * plane + (size_t)row * D_ + col);
  }
  float4 sum = make_float4(0.f, 0.f, 0.f, 0.f);
  #pragma unroll
  for (int k = 0; k < 4; ++k) {
    int row = rt * RT2 + rq + 16 * k;
    v[k].x = (t[k][0].x + t[k][1].x) + (t[k][2].x + t[k][3].x) + bv.x;
    v[k].y = (t[k][0].y + t[k][1].y) + (t[k][2].y + t[k][3].y) + bv.y;
    v[k].z = (t[k][0].z + t[k][1].z) + (t[k][2].z + t[k][3].z) + bv.z;
    v[k].w = (t[k][0].w + t[k][1].w) + (t[k][2].w + t[k][3].w) + bv.w;
    if (row < L_) {
      *(float4*)(y + (size_t)b * plane + (size_t)row * D_ + col) = v[k];
      sum.x += v[k].x; sum.y += v[k].y; sum.z += v[k].z; sum.w += v[k].w;
    }
  }
  s_red[rq][cq] = sum;
  __syncthreads();
  float4 m = make_float4(0.f, 0.f, 0.f, 0.f);
  #pragma unroll
  for (int i = 0; i < 16; ++i) { float4 q = s_red[i][cq]; m.x += q.x; m.y += q.y; m.z += q.z; m.w += q.w; }
  const float ic = 1.f / (float)cnt;
  m.x *= ic; m.y *= ic; m.z *= ic; m.w *= ic;
  float4 m2 = make_float4(0.f, 0.f, 0.f, 0.f);
  #pragma unroll
  for (int k = 0; k < 4; ++k) {
    int row = rt * RT2 + rq + 16 * k;
    if (row < L_) {
      float d;
      d = v[k].x - m.x; m2.x += d * d; d = v[k].y - m.y; m2.y += d * d;
      d = v[k].z - m.z; m2.z += d * d; d = v[k].w - m.w; m2.w += d * d;
    }
  }
  __syncthreads();
  s_red[rq][cq] = m2;
  __syncthreads();
  if (tid < 16) {
    float4 a = make_float4(0.f, 0.f, 0.f, 0.f);
    #pragma unroll
    for (int i = 0; i < 16; ++i) { float4 q = s_red[i][cq]; a.x += q.x; a.y += q.y; a.z += q.z; a.w += q.w; }
    float2* pp = part + ((size_t)b * NT2 + rt) * D_ + col;
    pp[0] = make_float2(m.x, a.x); pp[1] = make_float2(m.y, a.y); pp[2] = make_float2(m.z, a.z); pp[3] = make_float2(m.w, a.w);
  }
}

__global__ __launch_bounds__(256)
void norm_gelu_kernel(const float* __restrict__ y, const float2* __restrict__ part,
                      const __hip_bfloat16* __restrict__ gamma, const __hip_bfloat16* __restrict__ beta,
                      __hip_bfloat16* __restrict__ out) {
  __shared__ float s_scale[256], s_shift[256];
  const int tid = threadIdx.x;
  const int b = blockIdx.y;
  const int rows_per = 8;
  const int r0 = blockIdx.x * rows_per;
  const int c4 = (tid & 63) * 4;
  float4 v[2];
  #pragma unroll
  for (int k = 0; k < 2; ++k) {
    int rr = r0 + (tid >> 6) + 4 * k; rr = rr < L_ ? rr : L_ - 1;
    v[k] = *(const float4*)(y + ((size_t)b * L_ + rr) * D_ + c4);
  }
  {
    float2 pm[NT2];
    #pragma unroll
    for (int i = 0; i < NT2; ++i) pm[i] = part[((size_t)b * NT2 + i) * D_ + tid];
    float s = 0.f;
    #pragma unroll
    for (int i = 0; i < NT2 - 1; ++i) s += pm[i].x;
    const float nl = (float)(L_ - (NT2 - 1) * RT2);
    float mean = (s * (float)RT2 + pm[NT2 - 1].x * nl) * (1.f / (float)L_);
    float m2 = 0.f;
    #pragma unroll
    for (int i = 0; i < NT2 - 1; ++i) { float d = pm[i].x - mean; m2 += pm[i].y + (float)RT2 * d * d; }
    { float d = pm[NT2 - 1].x - mean; m2 += pm[NT2 - 1].y + nl * d * d; }
    float var = m2 * (1.f / (float)L_);
    float rs = rsqrtf(var + 1e-5f);
    float g = __bfloat162float(gamma[tid]), be = __bfloat162float(beta[tid]);
    s_scale[tid] = rs * g;
    s_shift[tid] = be - mean * rs * g;
  }
  __syncthreads();
  #pragma unroll
  for (int k = 0; k < 2; ++k) {
    int rr = r0 + (tid >> 6) + 4 * k;
    if (rr >= L_) break;
    float o[4] = {v[k].x, v[k].y, v[k].z, v[k].w};
    ushort4 ov;
    unsigned short* op = (unsigned short*)&ov;
    #pragma unroll
    for (int i = 0; i < 4; ++i) {
      float z = o[i] * s_scale[c4 + i] + s_shift[c4 + i];
      float u = 0.7978845608028654f * (z + 0.044715f * z * z * z);
      float t = tanhf(u);
      float g = 0.5f * z * (1.f + t);
      __hip_bfloat16 hb = __float2bfloat16(g);
      op[i] = *(unsigned short*)&hb;
    }
    *(ushort4*)(out + ((size_t)b * L_ + rr) * D_ + c4) = ov;
  }
}

extern "C" void kernel_launch(void* const* d_in, const int* in_sizes, int n_in,
                              void* d_out, int out_size, void* d_ws, size_t ws_size,
                              hipStream_t stream) {
  const __hip_bfloat16* x = (const __hip_bfloat16*)d_in[0];
  const __hip_bfloat16* w = (const __hip_bfloat16*)d_in[1];
  const __hip_bfloat16* bias = (const __hip_bfloat16*)d_in[2];
  const __hip_bfloat16* gamma = (const __hip_bfloat16*)d_in[3];
  const __hip_bfloat16* beta = (const __hip_bfloat16*)d_in[4];
  __hip_bfloat16* out = (__hip_bfloat16*)d_out;
  const size_t plane = (size_t)L_ * D_ * 4;
  float* yp = (float*)d_ws;
  float* y = (float*)((char*)d_ws + 8 * plane);
  float2* part = (float2*)((char*)d_ws + 10 * plane);
  (void)hipFuncSetAttribute((const void*)conv_gemm_kernel, hipFuncAttributeMaxDynamicSharedMemorySize, LDS_BYTES);
  conv_gemm_kernel<<<256, 256, LDS_BYTES, stream>>>(x, w, yp);
  reduce_stats_kernel<<<dim3(NT2 * 4, 2), 256, 0, stream>>>(yp, bias, y, part);
  dim3 g2((L_ + 7) / 8, 2);
  norm_gelu_kernel<<<g2, 256, 0, stream>>>(y, part, gamma, beta, out);
}
```

```cpp
#include <hip/hip_runtime.h>
#include <hip/hip_bf16.h>

typedef __attribute__((ext_vector_type(8))) short bf16x8;
typedef __attribute__((ext_vector_type(4))) short bf16x4;
typedef __attribute__((ext_vector_type(16))) float f32x16;

constexpr int T_ = 4000, D_ = 256, L_ = 1985;
constexpr int BM = 256, BN = 64;
constexpr int XR = 272;
constexpr int XBLK = XR * 128;
constexpr int WST = 8192;
constexpr int RW = 8;
constexpr int WOFF = 2 * XBLK;
constexpr int LDS_BYTES = WOFF + RW * WST;
constexpr int RT2 = 64;
constexpr int NT2 = 32;

struct Frags { bf16x8 a[2]; bf16x4 b[4]; };

template <int CB, int J, int KC, int PART>
__device__ __forceinline__ void read_frags(Frags& f, unsigned aaddr, unsigned wb0, unsigned wb1) {
  constexpr int AO = CB * XBLK;
  constexpr int WO = (J % RW) * WST + KC * 2048;
  if constexpr (PART == 0)
    asm volatile("ds_read_b128 %0, %2 offset:%4\n\tds_read_b64_tr_b16 %1, %3 offset:%5"
      : "=&v"(f.a[0]), "=&v"(f.b[0]) : "v"(aaddr), "v"(wb0), "i"(AO), "i"(WO) : "memory");
  else if constexpr (PART == 1)
    asm volatile("ds_read_b64_tr_b16 %1, %3 offset:%5+512\n\tds_read_b128 %0, %2 offset:%4+4096"
      : "=&v"(f.a[1]), "=&v"(f.b[1]) : "v"(aaddr), "v"(wb0), "i"(AO), "i"(WO) : "memory");
  else
    asm volatile("ds_read_b64_tr_b16 %0, %2 offset:%3\n\tds_read_b64_tr_b16 %1, %2 offset:%3+512"
      : "=&v"(f.b[2]), "=&v"(f.b[3]) : "v"(wb1), "i"(WO) : "memory");
}
template <int J, int KC>
__device__ __forceinline__ unsigned a_addr(unsigned smb, int row, int h) {
  int rr = row + J;
  return smb + rr * 128 + ((((2 * KC) + h) ^ ((rr >> 1) & 7)) << 4);
}

struct Ctx {
  char* sm;
  const char* wsrc;
  unsigned smb, wb0, wb1;
  int wid, p, row, h;
};

template <int V>
__device__ __forceinline__ void load_w(const Ctx& c) {
  constexpr int cb = V / 16, j = V % 16;
  if (V >= 32) return;
  if (j == 15 && c.p) return;
  const char* s = c.wsrc + (size_t)(2 * j * 256 + cb * 64) * 512;
  __builtin_amdgcn_global_load_lds((const unsigned*)s, (unsigned*)(c.sm + WOFF + (j % RW) * WST + c.wid * 2048), 16, 0, 0);
  __builtin_amdgcn_global_load_lds((const unsigned*)(s + 8 * 512), (unsigned*)(c.sm + WOFF + (j % RW) * WST + c.wid * 2048 + 1024), 16, 0, 0);
}

#define B8(F, N) __builtin_shufflevector(F.b[2 * N], F.b[2 * N + 1], 0, 1, 2, 3, 4, 5, 6, 7)
#define MFMA(M, N, F) acc[M][N] = __builtin_amdgcn_mfma_f32_32x32x16_bf16(F.a[M], B8(F, N), acc[M][N], 0, 0, 0)
#define LGKM(NN, F) asm volatile("s_waitcnt lgkmcnt(" #NN ")" : "+v"(F.a[0]), "+v"(F.a[1]), "+v"(F.b[0]), "+v"(F.b[1]), "+v"(F.b[2]), "+v"(F.b[3]) :: "memory")
#define MFMA4(F) MFMA(0, 0, F); MFMA(1, 0, F); MFMA(0, 1, F); MFMA(1, 1, F);

#define RDP(F, CBX, JX, KCX, P) read_frags<CBX, JX, KCX, P>(F, aa, c.wb0, c.wb1)
#define RD(F, CBX, JX, KCX) { unsigned aa = a_addr<JX, KCX>(c.smb, c.row, c.h); RDP(F, CBX, JX, KCX, 0); RDP(F, CBX, JX, KCX, 1); RDP(F, CBX, JX, KCX, 2); }
#define SB __builtin_amdgcn_sched_barrier(0)
#define KSTEP(F, NW, G, CBX, JX, KCX) { \
    int row_ = c.row; asm volatile("" : "+v"(row_)); \
    unsigned aa = a_addr<JX, KCX>(c.smb, row_, c.h); \
    LGKM(NW, F); MFMA(0, 0, F); SB; RDP(G, CBX, JX, KCX, 0); \
    MFMA(1, 0, F); SB; RDP(G, CBX, JX, KCX, 1); \
    MFMA(0, 1, F); SB; RDP(G, CBX, JX, KCX, 2); \
    MFMA(1, 1, F); SB; }
#define KLAST(F, NW) LGKM(NW, F); MFMA(0, 0, F); MFMA(1, 0, F); MFMA(0, 1, F); MFMA(1, 1, F); SB;
template <int V>
__device__ __forceinline__ void run_stage(const Ctx& c, Frags (&f)[4], f32x16 (&acc)[2][2]) {
  constexpr int CB = V / 16, J = V % 16;
  const bool skip = (J == 15) && c.p;
  if (!skip) {
    const bool lastp = (V == 31) || (V == 30 && c.p);
    if (!lastp) __builtin_amdgcn_s_barrier();
    KSTEP(f[0], 6, f[2], CB, J, 2)
    KSTEP(f[1], 6, f[3], CB, J, 3)
    if (lastp) {
      KLAST(f[2], 6)
      KLAST(f[3], 0)
    } else {
      constexpr int NCB = (J == 15) ? ((CB + 1) & 1) : CB, NJ = (J + 1) % 16;
      if (J == 14 && c.p) {
        KSTEP(f[2], 6, f[0], (CB + 1) & 1, 0, 0)
        KSTEP(f[3], 6, f[1], (CB + 1) & 1, 0, 1)
      } else {
        KSTEP(f[2], 6, f[0], NCB, NJ, 0)
        KSTEP(f[3], 6, f[1], NCB, NJ, 1)
      }
    }
  }
  if constexpr (V < 31) run_stage<V + 1>(c, f, acc);
}
template <int V>
__device__ __forceinline__ void run_loader(const Ctx& c) {
  constexpr int J = V % 16;
  const bool skip = (J == 15) && c.p;
  if (!skip) {
    const bool lastp = (V == 31) || (V == 30 && c.p);
    if (!lastp) {
      if constexpr (V < 6) asm volatile("s_waitcnt vmcnt(16)" ::: "memory");
      else if constexpr (V < 24) asm volatile("s_waitcnt vmcnt(8)" ::: "memory");
      else asm volatile("s_waitcnt vmcnt(0)" ::: "memory");
      __builtin_amdgcn_s_barrier();
      if constexpr (V == 16) { if (c.p) load_w<22>(c); }
      load_w<V + 7>(c);
    }
  }
  if constexpr (V < 31) run_loader<V + 1>(c);
}

__global__ __launch_bounds__(512, 1)
void conv_gemm_kernel(const __hip_bfloat16* __restrict__ x, const __hip_bfloat16* __restrict__ w,
                      float* __restrict__ yp) {
  extern __shared__ __attribute__((aligned(16))) char sm[];
  const int tid = threadIdx.x, lane = tid & 63;
  const int wid8 = __builtin_amdgcn_readfirstlane(tid >> 6);
  const int wid = wid8 & 3;
  const int bid = blockIdx.x;
  const int nt = (bid & 7) >> 1, p = bid & 1, rest = bid >> 3;
  const int chh = rest & 1, bm = rest >> 1;
  const int b = bm >> 3, mt = bm & 7;
  const int t0 = 2 * mt * BM;
  const int r = lane & 31, h = lane >> 5;

  Ctx c;
  c.sm = sm; c.wid = wid; c.p = p;
  if (wid8 >= 4) {
    const char* xb = (const char*)(x + (size_t)b * T_ * D_) + chh * 256;
    {
      int row = wid * 16 + (lane >> 3);
      int pc = lane & 7;
      int lch = (((pc >> 2) ^ ((row >> 1) & 1)) << 2) | (pc & 3);
      c.wsrc = (const char*)(w + (size_t)(p * 256 + chh * 128 + row) * D_ + nt * BN + lch * 8);
    }
    auto ldx = [&](int i) {
      int q = wid + 4 * i;
      int cb = q >= 34 ? 1 : 0;
      int rl = (q - cb * 34) * 8 + (lane >> 3);
      int ch = (lane & 7) ^ ((rl >> 1) & 7);
      int xrow = t0 + 2 * rl + p; xrow = xrow < T_ ? xrow : T_ - 1;
      __builtin_amdgcn_global_load_lds((const unsigned*)(xb + (size_t)xrow * 512 + cb * 128 + ch * 16), (unsigned*)(sm + q * 1024), 16, 0, 0);
    };
    #pragma unroll
    for (int i = 0; i < 9; ++i) ldx(i);
    load_w<0>(c); load_w<1>(c); load_w<2>(c); load_w<3>(c); load_w<4>(c); load_w<5>(c); load_w<6>(c);
    #pragma unroll
    for (int i = 9; i < 17; ++i) ldx(i);
    asm volatile("s_waitcnt vmcnt(20)" ::: "memory");
    __builtin_amdgcn_s_barrier();
    run_loader<0>(c);
    return;
  }
  const unsigned smb = (unsigned)(size_t)sm;
  c.smb = smb; c.row = wid * 64 + r; c.h = h;
  {
    int q = (lane & 15) >> 2;
    unsigned base = smb + WOFF + (8 * h + q) * 128 + ((lane >> 4) & 1) * 32 + (lane & 3) * 8;
    c.wb0 = base + (((q >> 1) & 1) << 6);
    c.wb1 = base + ((((q >> 1) & 1) ^ 1) << 6);
  }
  __builtin_amdgcn_s_barrier();
  f32x16 acc[2][2] = {};
  Frags f[4];
  RD(f[0], 0, 0, 0); RD(f[1], 0, 0, 1);
  run_stage<0>(c, f, acc);

  const int split = chh * 2 + p;
  float* o = yp + ((size_t)split * 2 + b) * ((size_t)L_ * D_);
  #pragma unroll
  for (int m = 0; m < 2; ++m) {
    const int row0 = mt * BM + wid * 64 + m * 32 + 4 * h;
    #pragma unroll
    for (int n = 0; n < 2; ++n) {
      const int col = nt * BN + n * 32 + r;
      #pragma unroll
      for (int i = 0; i < 16; ++i) {
        int row = row0 + (i & 3) + 8 * (i >> 2);
        if (row < L_) o[(size_t)row * D_ + col] = acc[m][n][i];
      }
    }
  }
}

__global__ __launch_bounds__(256)
void reduce_stats_kernel(const float* __restrict__ yp, const __hip_bfloat16* __restrict__ bias,
                         float* __restrict__ y, float2* __restrict__ part) {
  __shared__ float4 s_red[16][16];
  const int tid = threadIdx.x;
  const int ct = blockIdx.x & 3, rt = blockIdx.x >> 2, b = blockIdx.y;
  const int cq = tid & 15, rq = tid >> 4;
  const int col = ct * 64 + cq * 4;
  const size_t plane = (size_t)L_ * D_;
  float4 v[4];
  int cnt = L_ - rt * RT2; cnt = cnt > RT2 ? RT2 : cnt;
  float4 bv = make_float4(__bfloat162float(bias[col]), __bfloat162float(bias[col + 1]), __bfloat162float(bias[col + 2]), __bfloat162float(bias[col + 3]));
  float4 t[4][4];
  #pragma unroll
  for (int k = 0; k < 4; ++k) {
    int row = rt * RT2 + rq + 16 * k; row = row < L_ ? row : L_ - 1;
    #pragma unroll
    for (int s = 0; s < 4; ++s) t[k][s] = *(const float4*)(yp + ((size_t)s * 2 + b) * plane + (size_t)row * D_ + col);
  }
  float4 sum = make_float4(0.f, 0.f, 0.f, 0.f);
  #pragma unroll
  for (int k = 0; k < 4; ++k) {
    int row = rt * RT2 + rq + 16 * k;
    v[k].x = (t[k][0].x + t[k][1].x) + (t[k][2].x + t[k][3].x) + bv.x;
    v[k].y = (t[k][0].y + t[k][1].y) + (t[k][2].y + t[k][3].y) + bv.y;
    v[k].z = (t[k][0].z + t[k][1].z) + (t[k][2].z + t[k][3].z) + bv.z;
    v[k].w = (t[k][0].w + t[k][1].w) + (t[k][2].w + t[k][3].w) + bv.w;
    if (row < L_) {
      *(float4*)(y + (size_t)b * plane + (size_t)row * D_ + col) = v[k];
      sum.x += v[k].x; sum.y += v[k].y; sum.z += v[k].z; sum.w += v[k].w;
    }
  }
  s_red[rq][cq] = sum;
  __syncthreads();
  float4 m = make_float4(0.f, 0.f, 0.f, 0.f);
  #pragma unroll
  for (int i = 0; i < 16; ++i) { float4 q = s_red[i][cq]; m.x += q.x; m.y += q.y; m.z += q.z; m.w += q.w; }
  const float ic = 1.f / (float)cnt;
  m.x *= ic; m.y *= ic; m.z *= ic; m.w *= ic;
  float4 m2 = make_float4(0.f, 0.f, 0.f, 0.f);
  #pragma unroll
  for (int k = 0; k < 4; ++k) {
    int row = rt * RT2 + rq + 16 * k;
    if (row < L_) {
      float d;
      d = v[k].x - m.x; m2.x += d * d; d = v[k].y - m.y; m2.y += d * d;
      d = v[k].z - m.z; m2.z += d * d; d = v[k].w - m.w; m2.w += d * d;
    }
  }
  __syncthreads();
  s_red[rq][cq] = m2;
  __syncthreads();
  if (tid < 16) {
    float4 a = make_float4(0.f, 0.f, 0.f, 0.f);
    #pragma unroll
    for (int i = 0; i < 16; ++i) { float4 q = s_red[i][cq]; a.x += q.x; a.y += q.y; a.z += q.z; a.w += q.w; }
    float2* pp = part + ((size_t)b * NT2 + rt) * D_ + col;
    pp[0] = make_float2(m.x, a.x); pp[1] = make_float2(m.y, a.y); pp[2] = make_float2(m.z, a.z); pp[3] = make_float2(m.w, a.w);
  }
}

__global__ __launch_bounds__(256)
void norm_gelu_kernel(const float* __restrict__ y, const float2* __restrict__ part,
                      const __hip_bfloat16* __restrict__ gamma, const __hip_bfloat16* __restrict__ beta,
                      __hip_bfloat16* __restrict__ out) {
  __shared__ float s_scale[256], s_shift[256];
  const int tid = threadIdx.x;
  const int b = blockIdx.y;
  const int rows_per = 16;
  const int r0 = blockIdx.x * rows_per;
  const int c4 = (tid & 63) * 4;
  float4 v[4];
  #pragma unroll
  for (int k = 0; k < 4; ++k) {
    int rr = r0 + (tid >> 6) + 4 * k; rr = rr < L_ ? rr : L_ - 1;
    v[k] = *(const float4*)(y + ((size_t)b * L_ + rr) * D_ + c4);
  }
  {
    float2 pm[NT2];
    #pragma unroll
    for (int i = 0; i < NT2; ++i) pm[i] = part[((size_t)b * NT2 + i) * D_ + tid];
    float s = 0.f;
    #pragma unroll
    for (int i = 0; i < NT2 - 1; ++i) s += pm[i].x;
    const float nl = (float)(L_ - (NT2 - 1) * RT2);
    float mean = (s * (float)RT2 + pm[NT2 - 1].x * nl) * (1.f / (float)L_);
    float m2 = 0.f;
    #pragma unroll
    for (int i = 0; i < NT2 - 1; ++i) { float d = pm[i].x - mean; m2 += pm[i].y + (float)RT2 * d * d; }
    { float d = pm[NT2 - 1].x - mean; m2 += pm[NT2 - 1].y + nl * d * d; }
    float var = m2 * (1.f / (float)L_);
    float rs = rsqrtf(var + 1e-5f);
    float g = __bfloat162float(gamma[tid]), be = __bfloat162float(beta[tid]);
    s_scale[tid] = rs * g;
    s_shift[tid] = be - mean * rs * g;
  }
  __syncthreads();
  #pragma unroll
  for (int k = 0; k < 4; ++k) {
    int rr = r0 + (tid >> 6) + 4 * k;
    if (rr >= L_) break;
    float o[4] = {v[k].x, v[k].y, v[k].z, v[k].w};
    ushort4 ov;
    unsigned short* op = (unsigned short*)&ov;
    #pragma unroll
    for (int i = 0; i < 4; ++i) {
      float z = o[i] * s_scale[c4 + i] + s_shift[c4 + i];
      float u = 0.7978845608028654f * (z + 0.044715f * z * z * z);
      float t = tanhf(u);
      float g = 0.5f * z * (1.f + t);
      __hip_bfloat16 hb = __float2bfloat16(g);
      op[i] = *(unsigned short*)&hb;
    }
    *(ushort4*)(out + ((size_t)b * L_ + rr) * D_ + c4) = ov;
  }
}

extern "C" void kernel_launch(void* const* d_in, const int* in_sizes, int n_in,
                              void* d_out, int out_size, void* d_ws, size_t ws_size,
                              hipStream_t stream) {
  const __hip_bfloat16* x = (const __hip_bfloat16*)d_in[0];
  const __hip_bfloat16* w = (const __hip_bfloat16*)d_in[1];
  const __hip_bfloat16* bias = (const __hip_bfloat16*)d_in[2];
  const __hip_bfloat16* gamma = (const __hip_bfloat16*)d_in[3];
  const __hip_bfloat16* beta = (const __hip_bfloat16*)d_in[4];
  __hip_bfloat16* out = (__hip_bfloat16*)d_out;
  const size_t plane = (size_t)L_ * D_ * 4;
  float* yp = (float*)d_ws;
  float* y = (float*)((char*)d_ws + 8 * plane);
  float2* part = (float2*)((char*)d_ws + 10 * plane);
  (void)hipFuncSetAttribute((const void*)conv_gemm_kernel, hipFuncAttributeMaxDynamicSharedMemorySize, LDS_BYTES);
  conv_gemm_kernel<<<256, 512, LDS_BYTES, stream>>>(x, w, yp);
  reduce_stats_kernel<<<dim3(NT2 * 4, 2), 256, 0, stream>>>(yp, bias, y, part);
  dim3 g2((L_ + 15) / 16, 2);
  norm_gelu_kernel<<<g2, 256, 0, stream>>>(y, part, gamma, beta, out);
}
```

```cpp
#include <hip/hip_runtime.h>
#include <hip/hip_bf16.h>

typedef __attribute__((ext_vector_type(8))) short bf16x8;
typedef __attribute__((ext_vector_type(4))) short bf16x4;
typedef __attribute__((ext_vector_type(16))) float f32x16;

constexpr int T_ = 4000, D_ = 256, KT = 31, L_ = 1985;
constexpr int BM = 128, BN = 32;
constexpr int MT = 16;
constexpr int XR = 144;
constexpr int XPAR = XR * 128;
constexpr int XBLK = 2 * XPAR;
constexpr int WST = 8192;
constexpr int RW = 8;
constexpr int LDS_BYTES = 2 * XBLK + RW * WST;

struct Frags { bf16x8 a; bf16x4 b0, b1; };

template <int J, int P, int KC>
__device__ __forceinline__ void read_frag(Frags& f, unsigned xbase, int row, int h, unsigned wb) {
  constexpr int WO = (J % RW) * WST + P * 4096 + KC * 1024;
  int rr = row + J;
  unsigned aa = xbase + rr * 128 + ((((2 * KC) + h) ^ ((rr >> 1) & 7)) << 4);
  asm volatile(
      "ds_read_b128 %0, %3 offset:%5\n\t"
      "ds_read_b64_tr_b16 %1, %4 offset:%6\n\t"
      "ds_read_b64_tr_b16 %2, %4 offset:%6+256\n\t"
      : "=&v"(f.a), "=&v"(f.b0), "=&v"(f.b1)
      : "v"(aa), "v"(wb), "i"(P * XPAR), "i"(WO)
      : "memory");
}

struct Ctx {
  char* sm;
  const char* wsrc;
  const char* xsrc[9];
  unsigned xb, xbn, wb;
  int wid, row, h;
  int xdst;
  bool more;
};

template <int V>
__device__ __forceinline__ void load_w(const Ctx& c) {
  constexpr int nb = V / 16, j = V % 16;
  if (j == 15 && c.wid >= 2) return;
  const char* s = c.wsrc + (size_t)(2 * j * 256 + nb * 64) * 512;
  __builtin_amdgcn_global_load_lds((const unsigned*)s, (unsigned*)(c.sm + 2 * XBLK + (j % RW) * WST + c.wid * 2048), 16, 0, 0);
  __builtin_amdgcn_global_load_lds((const unsigned*)(s + 16 * 512), (unsigned*)(c.sm + 2 * XBLK + (j % RW) * WST + c.wid * 2048 + 1024), 16, 0, 0);
}
template <int I>
__device__ __forceinline__ void load_x(const Ctx& c) {
  __builtin_amdgcn_global_load_lds((const unsigned*)(c.xsrc[I]), (unsigned*)(c.sm + c.xdst + (c.wid + 4 * I) * 1024), 16, 0, 0);
}

template <int J>
__device__ __forceinline__ void run_loader(const Ctx& c) {
  const bool last = (J == 15) && !c.more;
  if (!last) {
    if constexpr (J < 9) asm volatile("s_waitcnt vmcnt(8)" ::: "memory");
    else { if (c.more) asm volatile("s_waitcnt vmcnt(8)" ::: "memory"); else asm volatile("s_waitcnt vmcnt(0)" ::: "memory"); }
    __builtin_amdgcn_s_barrier();
    if constexpr (J + 7 < 16) load_w<J + 7>(c);
    else { if (c.more) load_w<J + 7>(c); }
    if constexpr (J < 9) { if (c.more) load_x<J>(c); }
  }
  if constexpr (J < 15) run_loader<J + 1>(c);
}

#define MFMA(F) acc = __builtin_amdgcn_mfma_f32_32x32x16_bf16(F.a, __builtin_shufflevector(F.b0, F.b1, 0, 1, 2, 3, 4, 5, 6, 7), acc, 0, 0, 0)
#define WAITF(N, F) asm volatile("s_waitcnt lgkmcnt(" #N ")" : "+v"(F.a), "+v"(F.b0), "+v"(F.b1) :: "memory")
#define SB __builtin_amdgcn_sched_barrier(0)
#define STEP(KC, NJ, NP, XB) { WAITF(9, f[KC]); MFMA(f[KC]); SB; int row_ = c.row; asm volatile("" : "+v"(row_)); \
    read_frag<NJ, NP, KC>(f[KC], XB, row_, c.h, c.wb); }
#define HALF(NJ, NP, XB) STEP(0, NJ, NP, XB) STEP(1, NJ, NP, XB) STEP(2, NJ, NP, XB) STEP(3, NJ, NP, XB)

template <int J>
__device__ __forceinline__ void run_stage(const Ctx& c, Frags (&f)[4], f32x16& acc) {
  const bool last = (J == 15) && !c.more;
  if (!last) {
    __builtin_amdgcn_s_barrier();
    if constexpr (J < 15) {
      HALF(J, 1, c.xb)
      HALF(J + 1, 0, c.xb)
    } else {
      HALF(0, 0, c.xbn)
    }
  } else {
    WAITF(9, f[0]); MFMA(f[0]); WAITF(6, f[1]); MFMA(f[1]); WAITF(3, f[2]); MFMA(f[2]); WAITF(0, f[3]); MFMA(f[3]);
  }
  SB;
  if constexpr (J < 15) run_stage<J + 1>(c, f, acc);
}

__global__ __launch_bounds__(512, 1)
void conv_gemm_kernel(const __hip_bfloat16* __restrict__ x, const __hip_bfloat16* __restrict__ w,
                      const __hip_bfloat16* __restrict__ bias, float* __restrict__ y,
                      float2* __restrict__ part) {
  extern __shared__ __attribute__((aligned(16))) char sm[];
  const int tid = threadIdx.x, lane = tid & 63;
  const int wid8 = __builtin_amdgcn_readfirstlane(tid >> 6);
  const int wq = wid8 & 3;
  const int bid = blockIdx.x;
  const int nt = bid & 7, rest = bid >> 3;
  const int b = rest >> 4, mt = rest & 15;
  const int t0 = 2 * mt * BM;
  const int r = lane & 31, h = lane >> 5;

  Ctx c;
  c.sm = sm; c.wid = wq;
  if (wid8 >= 4) {
    const char* xb = (const char*)(x + (size_t)b * T_ * D_);
    c.wsrc = (const char*)(w + (size_t)((wq >> 1) * 256 + (wq & 1) * 32 + (lane >> 2)) * D_ + nt * BN + (lane & 3) * 8);
    #pragma unroll
    for (int i = 0; i < 9; ++i) {
      int q = wq + 4 * i;
      int p = q >= 18 ? 1 : 0;
      int rl = (q - p * 18) * 8 + (lane >> 3);
      int ch = (lane & 7) ^ ((rl >> 1) & 7);
      int xrow = t0 + 2 * rl + p; xrow = xrow < T_ ? xrow : T_ - 1;
      c.xsrc[i] = xb + (size_t)xrow * 512 + ch * 16;
    }
    c.xdst = 0;
    load_x<0>(c); load_x<1>(c); load_x<2>(c); load_x<3>(c); load_x<4>(c);
    load_x<5>(c); load_x<6>(c); load_x<7>(c); load_x<8>(c);
    load_w<0>(c); load_w<1>(c); load_w<2>(c); load_w<3>(c); load_w<4>(c); load_w<5>(c); load_w<6>(c);
    asm volatile("s_waitcnt vmcnt(12)" ::: "memory");
    __builtin_amdgcn_s_barrier();
    #pragma unroll 1
    for (int cb = 0; cb < 4; ++cb) {
      c.more = cb < 3;
      c.xdst = ((cb + 1) & 1) * XBLK;
      #pragma unroll
      for (int i = 0; i < 9; ++i) c.xsrc[i] += 128;
      run_loader<0>(c);
      c.wsrc += 64 * 512;
    }
    return;
  }
  const unsigned smb = (unsigned)(size_t)sm;
  c.xb = smb; c.xbn = smb + XBLK; c.row = wq * 32 + r; c.h = h;
  c.wb = smb + 2 * XBLK + (8 * h + ((lane & 15) >> 2)) * 64 + (((lane >> 4) & 1) * 16 + (lane & 3) * 4) * 2;
  __builtin_amdgcn_s_barrier();
  f32x16 acc = {};
  Frags f[4];
  read_frag<0, 0, 0>(f[0], c.xb, c.row, h, c.wb); read_frag<0, 0, 1>(f[1], c.xb, c.row, h, c.wb);
  read_frag<0, 0, 2>(f[2], c.xb, c.row, h, c.wb); read_frag<0, 0, 3>(f[3], c.xb, c.row, h, c.wb);
  #pragma unroll 1
  for (int cb = 0; cb < 4; ++cb) {
    c.more = cb < 3;
    run_stage<0>(c, f, acc);
    unsigned u = c.xb; c.xb = c.xbn; c.xbn = u;
  }

  __syncthreads();
  const int col = nt * BN + r;
  const int row0 = mt * BM + wq * 32;
  int cnt = L_ - row0; cnt = cnt > 32 ? 32 : (cnt < 0 ? 0 : cnt);
  int cnt_wg = L_ - mt * BM; cnt_wg = cnt_wg > BM ? BM : cnt_wg;
  float* st = (float*)(sm + 32768);
  {
    const float bv = __bfloat162float(bias[col]);
    float* yp = y + ((size_t)b * L_ + row0 + 4 * h) * D_ + col;
    float sum = 0.f;
    #pragma unroll
    for (int i = 0; i < 16; ++i) {
      int rr = (i & 3) + 8 * (i >> 2) + 4 * h;
      float v = acc[i] + bv; acc[i] = v;
      if (rr < cnt) { sum += v; yp[((i & 3) + 8 * (i >> 2)) * D_] = v; }
    }
    sum += __shfl_xor(sum, 32);
    if (h == 0) st[wq * 32 + r] = sum;
  }
  __syncthreads();
  float mean = 0.f;
  {
    mean = (st[r] + st[32 + r] + st[64 + r] + st[96 + r]) / (float)cnt_wg;
    float m2 = 0.f;
    #pragma unroll
    for (int i = 0; i < 16; ++i) {
      int rr = (i & 3) + 8 * (i >> 2) + 4 * h;
      float d = acc[i] - mean;
      m2 += (rr < cnt) ? d * d : 0.f;
    }
    m2 += __shfl_xor(m2, 32);
    if (h == 0) st[128 + wq * 32 + r] = m2;
  }
  __syncthreads();
  if (tid < 32)
    part[((size_t)b * MT + mt) * D_ + col] = make_float2(mean, st[128 + r] + st[160 + r] + st[192 + r] + st[224 + r]);
}

__global__ __launch_bounds__(256)
void norm_gelu_kernel(const float* __restrict__ y, const float2* __restrict__ part,
                      const __hip_bfloat16* __restrict__ gamma, const __hip_bfloat16* __restrict__ beta,
                      __hip_bfloat16* __restrict__ out) {
  __shared__ float s_scale[256], s_shift[256];
  const int tid = threadIdx.x;
  const int b = blockIdx.y;
  const int rows_per = 8;
  const int r0 = blockIdx.x * rows_per;
  const int c4 = (tid & 63) * 4;
  float4 v[2];
  #pragma unroll
  for (int k = 0; k < 2; ++k) {
    int rr = r0 + (tid >> 6) + 4 * k; rr = rr < L_ ? rr : L_ - 1;
    v[k] = *(const float4*)(y + ((size_t)b * L_ + rr) * D_ + c4);
  }
  {
    float2 pm[16];
    #pragma unroll
    for (int i = 0; i < 16; ++i) pm[i] = part[((size_t)b * MT + i) * D_ + tid];
    float s = 0.f;
    #pragma unroll
    for (int i = 0; i < 15; ++i) s += pm[i].x;
    const float nl = (float)(L_ - 15 * BM);
    float mean = (s * (float)BM + pm[15].x * nl) * (1.f / (float)L_);
    float m2 = 0.f;
    #pragma unroll
    for (int i = 0; i < 15; ++i) { float d = pm[i].x - mean; m2 += pm[i].y + (float)BM * d * d; }
    { float d = pm[15].x - mean; m2 += pm[15].y + nl * d * d; }
    float var = m2 * (1.f / (float)L_);
    float rs = rsqrtf(var + 1e-5f);
    float g = __bfloat162float(gamma[tid]), be = __bfloat162float(beta[tid]);
    s_scale[tid] = rs * g;
    s_shift[tid] = be - mean * rs * g;
  }
  __syncthreads();
  #pragma unroll
  for (int k = 0; k < 2; ++k) {
    int rr = r0 + (tid >> 6) + 4 * k;
    if (rr >= L_) break;
    float o[4] = {v[k].x, v[k].y, v[k].z, v[k].w};
    ushort4 ov;
    unsigned short* op = (unsigned short*)&ov;
    #pragma unroll
    for (int i = 0; i < 4; ++i) {
      float z = o[i] * s_scale[c4 + i] + s_shift[c4 + i];
      float u = 0.7978845608028654f * (z + 0.044715f * z * z * z);
      float t = tanhf(u);
      float g = 0.5f * z * (1.f + t);
      __hip_bfloat16 hb = __float2bfloat16(g);
      op[i] = *(unsigned short*)&hb;
    }
    *(ushort4*)(out + ((size_t)b * L_ + rr) * D_ + c4) = ov;
  }
}

extern "C" void kernel_launch(void* const* d_in, const int* in_sizes, int n_in,
                              void* d_out, int out_size, void* d_ws, size_t ws_size,
                              hipStream_t stream) {
  const __hip_bfloat16* x = (const __hip_bfloat16*)d_in[0];
  const __hip_bfloat16* w = (const __hip_bfloat16*)d_in[1];
  const __hip_bfloat16* bias = (const __hip_bfloat16*)d_in[2];
  const __hip_bfloat16* gamma = (const __hip_bfloat16*)d_in[3];
  const __hip_bfloat16* beta = (const __hip_bfloat16*)d_in[4];
  __hip_bfloat16* out = (__hip_bfloat16*)d_out;
  float* y = (float*)d_ws;
  float2* part = (float2*)((char*)d_ws + (size_t)2 * L_ * D_ * 4);
  (void)hipFuncSetAttribute((const void*)conv_gemm_kernel, hipFuncAttributeMaxDynamicSharedMemorySize, LDS_BYTES);
  conv_gemm_kernel<<<256, 512, LDS_BYTES, stream>>>(x, w, bias, y, part);
  dim3 g2((L_ + 7) / 8, 2);
  norm_gelu_kernel<<<g2, 256, 0, stream>>>(y, part, gamma, beta, out);
}
```

```cpp
#include <hip/hip_runtime.h>
#include <hip/hip_bf16.h>

typedef __attribute__((ext_vector_type(8))) short bf16x8;
typedef __attribute__((ext_vector_type(4))) short bf16x4;
typedef __attribute__((ext_vector_type(16))) float f32x16;

constexpr int T_ = 4000, D_ = 256, KT = 31, L_ = 1985;
constexpr int BM = 128, BN = 32;
constexpr int MT = 16;
constexpr int XR = 144;
constexpr int XPAR = XR * 128;
constexpr int XBLK = 2 * XPAR;
constexpr int WST = 8192;
constexpr int RW = 8;
constexpr int LDS_BYTES = 2 * XBLK + RW * WST;

struct Frags { bf16x8 a; bf16x4 b0, b1; };

template <int J, int KC>
__device__ __forceinline__ unsigned a_addr(unsigned xbase, int row, int h) {
  int rr = row + J;
  return xbase + rr * 128 + ((((2 * KC) + h) ^ ((rr >> 1) & 7)) << 4);
}
template <int J, int P, int KC>
__device__ __forceinline__ void read_frag(Frags& f, unsigned aa, unsigned wb) {
  constexpr int WO = (J % RW) * WST + P * 4096 + KC * 1024;
  asm volatile(
      "ds_read_b128 %0, %3 offset:%5\n\t"
      "ds_read_b64_tr_b16 %1, %4 offset:%6\n\t"
      "ds_read_b64_tr_b16 %2, %4 offset:%6+256\n\t"
      : "=&v"(f.a), "=&v"(f.b0), "=&v"(f.b1)
      : "v"(aa), "v"(wb), "i"(P * XPAR + J * 16 + KC * 2 * XR * 16), "i"(WO)
      : "memory");
}

struct Ctx {
  char* sm;
  const char* wsrc;
  const char* xsrc[9];
  unsigned xb, xbn, wb;
  int wid, row, h;
  int xdst;
  bool more;
};

template <int V>
__device__ __forceinline__ void load_w(const Ctx& c) {
  constexpr int nb = V / 16, j = V % 16;
  if (j == 15 && c.wid >= 2) return;
  const char* s = c.wsrc + (size_t)(2 * j * 256 + nb * 64) * 512;
  __builtin_amdgcn_global_load_lds((const unsigned*)s, (unsigned*)(c.sm + 2 * XBLK + (j % RW) * WST + c.wid * 2048), 16, 0, 0);
  __builtin_amdgcn_global_load_lds((const unsigned*)(s + 16 * 512), (unsigned*)(c.sm + 2 * XBLK + (j % RW) * WST + c.wid * 2048 + 1024), 16, 0, 0);
}
template <int I>
__device__ __forceinline__ void load_x(const Ctx& c) {
  __builtin_amdgcn_global_load_lds((const unsigned*)(c.xsrc[I]), (unsigned*)(c.sm + c.xdst + (c.wid + 4 * I) * 1024), 16, 0, 0);
}

template <int J>
__device__ __forceinline__ void run_loader(const Ctx& c) {
  const bool last = (J == 15) && !c.more;
  if (!last) {
    if constexpr (J < 9) asm volatile("s_waitcnt vmcnt(8)" ::: "memory");
    else { if (c.more) asm volatile("s_waitcnt vmcnt(8)" ::: "memory"); else asm volatile("s_waitcnt vmcnt(0)" ::: "memory"); }
    __builtin_amdgcn_s_barrier();
    if constexpr (J + 7 < 16) load_w<J + 7>(c);
    else { if (c.more) load_w<J + 7>(c); }
    if constexpr (J < 9) { if (c.more) load_x<J>(c); }
  }
  if constexpr (J < 15) run_loader<J + 1>(c);
}

#define MFMA(F, K) acc[(K) & 1] = __builtin_amdgcn_mfma_f32_32x32x16_bf16(F.a, __builtin_shufflevector(F.b0, F.b1, 0, 1, 2, 3, 4, 5, 6, 7), acc[(K) & 1], 0, 0, 0)
#define WAITF(N, F) asm volatile("s_waitcnt lgkmcnt(" #N ")" : "+v"(F.a), "+v"(F.b0), "+v"(F.b1) :: "memory")
#define SB __builtin_amdgcn_sched_barrier(0)
#define STEP(KC, NJ, NP, XB) { WAITF(9, f[KC]); MFMA(f[KC], KC); SB; read_frag<NJ, NP, KC>(f[KC], XB, c.wb); }
#define HALF(NJ, NP, XB) STEP(0, NJ, NP, XB) STEP(1, NJ, NP, XB) STEP(2, NJ, NP, XB) STEP(3, NJ, NP, XB)

template <int J>
__device__ __forceinline__ void run_stage(const Ctx& c, Frags (&f)[4], f32x16 (&acc)[2]) {
  const bool last = (J == 15) && !c.more;
  if (!last) {
    __builtin_amdgcn_s_barrier();
    if constexpr (J < 15) {
      HALF(J, 1, c.xb)
      HALF(J + 1, 0, c.xb)
    } else {
      HALF(0, 0, c.xbn)
    }
  } else {
    WAITF(9, f[0]); MFMA(f[0], 0); WAITF(6, f[1]); MFMA(f[1], 1); WAITF(3, f[2]); MFMA(f[2], 2); WAITF(0, f[3]); MFMA(f[3], 3);
  }
  SB;
  if constexpr (J < 15) run_stage<J + 1>(c, f, acc);
}

__global__ __launch_bounds__(512, 1)
void conv_gemm_kernel(const __hip_bfloat16* __restrict__ x, const __hip_bfloat16* __restrict__ w,
                      const __hip_bfloat16* __restrict__ bias, float* __restrict__ y,
                      float2* __restrict__ part) {
  extern __shared__ __attribute__((aligned(16))) char sm[];
  const int tid = threadIdx.x, lane = tid & 63;
  const int wid8 = __builtin_amdgcn_readfirstlane(tid >> 6);
  const int wq = wid8 & 3;
  const int bid = blockIdx.x;
  const int nt = bid & 7, rest = bid >> 3;
  const int b = rest >> 4, mt = rest & 15;
  const int t0 = 2 * mt * BM;
  const int r = lane & 31, h = lane >> 5;

  Ctx c;
  c.sm = sm; c.wid = wq;
  if (wid8 >= 4) {
    const char* xb = (const char*)(x + (size_t)b * T_ * D_);
    c.wsrc = (const char*)(w + (size_t)((wq >> 1) * 256 + (wq & 1) * 32 + (lane >> 2)) * D_ + nt * BN + (lane & 3) * 8);
    #pragma unroll
    for (int i = 0; i < 9; ++i) {
      int q = wq + 4 * i;
      int e = q * 64 + lane;
      int p = e / (8 * XR); int rem = e - p * (8 * XR);
      int ch = rem / XR, rl = rem - ch * XR;
      int xrow = t0 + 2 * rl + p; xrow = xrow < T_ ? xrow : T_ - 1;
      c.xsrc[i] = xb + (size_t)xrow * 512 + ch * 16;
    }
    c.xdst = 0;
    load_x<0>(c); load_x<1>(c); load_x<2>(c); load_x<3>(c); load_x<4>(c);
    load_x<5>(c); load_x<6>(c); load_x<7>(c); load_x<8>(c);
    load_w<0>(c); load_w<1>(c); load_w<2>(c); load_w<3>(c); load_w<4>(c); load_w<5>(c); load_w<6>(c);
    asm volatile("s_waitcnt vmcnt(12)" ::: "memory");
    __builtin_amdgcn_s_barrier();
    #pragma unroll 1
    for (int cb = 0; cb < 4; ++cb) {
      c.more = cb < 3;
      c.xdst = ((cb + 1) & 1) * XBLK;
      #pragma unroll
      for (int i = 0; i < 9; ++i) c.xsrc[i] += 128;
      run_loader<0>(c);
      c.wsrc += 64 * 512;
    }
    return;
  }
  const unsigned smb = (unsigned)(size_t)sm;
  c.xb = smb + h * (XR * 16) + (wq * 32 + r) * 16; c.xbn = c.xb + XBLK;
  c.wb = smb + 2 * XBLK + (8 * h + ((lane & 15) >> 2)) * 64 + (((lane >> 4) & 1) * 16 + (lane & 3) * 4) * 2;
  __builtin_amdgcn_s_barrier();
  f32x16 acc2[2] = {};
  Frags f[4];
  read_frag<0, 0, 0>(f[0], c.xb, c.wb); read_frag<0, 0, 1>(f[1], c.xb, c.wb);
  read_frag<0, 0, 2>(f[2], c.xb, c.wb); read_frag<0, 0, 3>(f[3], c.xb, c.wb);
  #pragma unroll 1
  for (int cb = 0; cb < 4; ++cb) {
    c.more = cb < 3;
    run_stage<0>(c, f, acc2);
    unsigned u = c.xb; c.xb = c.xbn; c.xbn = u;
  }
  f32x16 acc = acc2[0] + acc2[1];

  __syncthreads();
  const int col = nt * BN + r;
  const int row0 = mt * BM + wq * 32;
  int cnt = L_ - row0; cnt = cnt > 32 ? 32 : (cnt < 0 ? 0 : cnt);
  int cnt_wg = L_ - mt * BM; cnt_wg = cnt_wg > BM ? BM : cnt_wg;
  float* st = (float*)(sm + 32768);
  {
    const float bv = __bfloat162float(bias[col]);
    float* yp = y + ((size_t)b * L_ + row0 + 4 * h) * D_ + col;
    float sum = 0.f;
    #pragma unroll
    for (int i = 0; i < 16; ++i) {
      int rr = (i & 3) + 8 * (i >> 2) + 4 * h;
      float v = acc[i] + bv; acc[i] = v;
      if (rr < cnt) { sum += v; yp[((i & 3) + 8 * (i >> 2)) * D_] = v; }
    }
    sum += __shfl_xor(sum, 32);
    if (h == 0) st[wq * 32 + r] = sum;
  }
  __syncthreads();
  float mean = 0.f;
  {
    mean = (st[r] + st[32 + r] + st[64 + r] + st[96 + r]) / (float)cnt_wg;
    float m2 = 0.f;
    #pragma unroll
    for (int i = 0; i < 16; ++i) {
      int rr = (i & 3) + 8 * (i >> 2) + 4 * h;
      float d = acc[i] - mean;
      m2 += (rr < cnt) ? d * d : 0.f;
    }
    m2 += __shfl_xor(m2, 32);
    if (h == 0) st[128 + wq * 32 + r] = m2;
  }
  __syncthreads();
  if (tid < 32)
    part[((size_t)b * MT + mt) * D_ + col] = make_float2(mean, st[128 + r] + st[160 + r] + st[192 + r] + st[224 + r]);
}

__global__ __launch_bounds__(256)
void norm_gelu_kernel(const float* __restrict__ y, const float2* __restrict__ part,
                      const __hip_bfloat16* __restrict__ gamma, const __hip_bfloat16* __restrict__ beta,
                      __hip_bfloat16* __restrict__ out) {
  __shared__ float s_scale[256], s_shift[256];
  const int tid = threadIdx.x;
  const int b = blockIdx.y;
  const int rows_per = 8;
  const int r0 = blockIdx.x * rows_per;
  const int c4 = (tid & 63) * 4;
  float4 v[2];
  #pragma unroll
  for (int k = 0; k < 2; ++k) {
    int rr = r0 + (tid >> 6) + 4 * k; rr = rr < L_ ? rr : L_ - 1;
    v[k] = *(const float4*)(y + ((size_t)b * L_ + rr) * D_ + c4);
  }
  {
    float2 pm[16];
    #pragma unroll
    for (int i = 0; i < 16; ++i) pm[i] = part[((size_t)b * MT + i) * D_ + tid];
    float s = 0.f;
    #pragma unroll
    for (int i = 0; i < 15; ++i) s += pm[i].x;
    const float nl = (float)(L_ - 15 * BM);
    float mean = (s * (float)BM + pm[15].x * nl) * (1.f / (float)L_);
    float m2 = 0.f;
    #pragma unroll
    for (int i = 0; i < 15; ++i) { float d = pm[i].x - mean; m2 += pm[i].y + (float)BM * d * d; }
    { float d = pm[15].x - mean; m2 += pm[15].y + nl * d * d; }
    float var = m2 * (1.f / (float)L_);
    float rs = rsqrtf(var + 1e-5f);
    float g = __bfloat162float(gamma[tid]), be = __bfloat162float(beta[tid]);
    s_scale[tid] = rs * g;
    s_shift[tid] = be - mean * rs * g;
  }
  __syncthreads();
  #pragma unroll
  for (int k = 0; k < 2; ++k) {
    int rr = r0 + (tid >> 6) + 4 * k;
    if (rr >= L_) break;
    float o[4] = {v[k].x, v[k].y, v[k].z, v[k].w};
    ushort4 ov;
    unsigned short* op = (unsigned short*)&ov;
    #pragma unroll
    for (int i = 0; i < 4; ++i) {
      float z = o[i] * s_scale[c4 + i] + s_shift[c4 + i];
      float u = 0.7978845608028654f * (z + 0.044715f * z * z * z);
      float t = tanhf(u);
      float g = 0.5f * z * (1.f + t);
      __hip_bfloat16 hb = __float2bfloat16(g);
      op[i] = *(unsigned short*)&hb;
    }
    *(ushort4*)(out + ((size_t)b * L_ + rr) * D_ + c4) = ov;
  }
}

extern "C" void kernel_launch(void* const* d_in, const int* in_sizes, int n_in,
                              void* d_out, int out_size, void* d_ws, size_t ws_size,
                              hipStream_t stream) {
  const __hip_bfloat16* x = (const __hip_bfloat16*)d_in[0];
  const __hip_bfloat16* w = (const __hip_bfloat16*)d_in[1];
  const __hip_bfloat16* bias = (const __hip_bfloat16*)d_in[2];
  const __hip_bfloat16* gamma = (const __hip_bfloat16*)d_in[3];
  const __hip_bfloat16* beta = (const __hip_bfloat16*)d_in[4];
  __hip_bfloat16* out = (__hip_bfloat16*)d_out;
  float* y = (float*)d_ws;
  float2* part = (float2*)((char*)d_ws + (size_t)2 * L_ * D_ * 4);
  (void)hipFuncSetAttribute((const void*)conv_gemm_kernel, hipFuncAttributeMaxDynamicSharedMemorySize, LDS_BYTES);
  conv_gemm_kernel<<<256, 512, LDS_BYTES, stream>>>(x, w, bias, y, part);
  dim3 g2((L_ + 7) / 8, 2);
  norm_gelu_kernel<<<g2, 256, 0, stream>>>(y, part, gamma, beta, out);
}
```

```cpp
#include <hip/hip_runtime.h>
#include <hip/hip_bf16.h>

typedef __attribute__((ext_vector_type(8))) short bf16x8;
typedef __attribute__((ext_vector_type(4))) short bf16x4;
typedef __attribute__((ext_vector_type(16))) float f32x16;

constexpr int T_ = 4000, D_ = 256, KT = 31, L_ = 1985;
constexpr int BM = 128, BN = 32;
constexpr int MT = 16;
constexpr int XR = 144;
constexpr int XPAR = XR * 128;
constexpr int XBLK = 2 * XPAR;
constexpr int WST = 8192;
constexpr int RW = 8;
constexpr int LDS_BYTES = 2 * XBLK + RW * WST;

struct Frags { bf16x8 a; bf16x4 b0, b1; };

template <int J, int KC>
__device__ __forceinline__ unsigned a_addr(unsigned xbase, int row, int h) {
  int rr = row + J;
  return xbase + rr * 128 + ((((2 * KC) + h) ^ ((rr >> 1) & 7)) << 4);
}
template <int J, int P, int KC>
__device__ __forceinline__ void read_frag(Frags& f, unsigned aa, unsigned wb) {
  constexpr int WO = (J % RW) * WST + P * 4096 + KC * 1024;
  asm volatile(
      "ds_read_b128 %0, %3 offset:%5\n\t"
      "ds_read_b64_tr_b16 %1, %4 offset:%6\n\t"
      "ds_read_b64_tr_b16 %2, %4 offset:%6+256\n\t"
      : "=&v"(f.a), "=&v"(f.b0), "=&v"(f.b1)
      : "v"(aa), "v"(wb), "i"(P * XPAR + J * 16 + KC * 2 * XR * 16), "i"(WO)
      : "memory");
}

struct Ctx {
  char* sm;
  const char* wsrc;
  const char* xsrc[9];
  unsigned xb, xbn, wb;
  int wid, row, h;
  int xdst;
  bool more;
};

template <int V>
__device__ __forceinline__ void load_w(const Ctx& c) {
  constexpr int nb = V / 16, j = V % 16;
  if (j == 15 && c.wid >= 2) return;
  const char* s = c.wsrc + (size_t)(2 * j * 256 + nb * 64) * 512;
  __builtin_amdgcn_global_load_lds((const unsigned*)s, (unsigned*)(c.sm + 2 * XBLK + (j % RW) * WST + c.wid * 2048), 16, 0, 0);
  __builtin_amdgcn_global_load_lds((const unsigned*)(s + 16 * 512), (unsigned*)(c.sm + 2 * XBLK + (j % RW) * WST + c.wid * 2048 + 1024), 16, 0, 0);
}
template <int I>
__device__ __forceinline__ void load_x(const Ctx& c) {
  __builtin_amdgcn_global_load_lds((const unsigned*)(c.xsrc[I]), (unsigned*)(c.sm + c.xdst + (c.wid + 4 * I) * 1024), 16, 0, 0);
}

template <int J>
__device__ __forceinline__ void run_loader(const Ctx& c) {
  const bool last = (J == 15) && !c.more;
  if (!last) {
    if constexpr (J < 9) asm volatile("s_waitcnt vmcnt(8)" ::: "memory");
    else { if (c.more) asm volatile("s_waitcnt vmcnt(8)" ::: "memory"); else asm volatile("s_waitcnt vmcnt(0)" ::: "memory"); }
    __builtin_amdgcn_s_barrier();
    if constexpr (J + 7 < 16) load_w<J + 7>(c);
    else { if (c.more) load_w<J + 7>(c); }
    if constexpr (J < 9) { if (c.more) load_x<J>(c); }
  }
  if constexpr (J < 15) run_loader<J + 1>(c);
}

#define MFMA(F, K) acc[(K) & 1] = __builtin_amdgcn_mfma_f32_32x32x16_bf16(F.a, __builtin_shufflevector(F.b0, F.b1, 0, 1, 2, 3, 4, 5, 6, 7), acc[(K) & 1], 0, 0, 0)
#define WAITF(N, F) asm volatile("s_waitcnt lgkmcnt(" #N ")" : "+v"(F.a), "+v"(F.b0), "+v"(F.b1) :: "memory")
#define SB __builtin_amdgcn_sched_barrier(0)
#define STEP(KC, NJ, NP, XB) { WAITF(9, f[KC]); MFMA(f[KC], KC); SB; read_frag<NJ, NP, KC>(f[KC], XB, c.wb); }
#define HALF(NJ, NP, XB) STEP(0, NJ, NP, XB) STEP(1, NJ, NP, XB) STEP(2, NJ, NP, XB) STEP(3, NJ, NP, XB)

template <int J>
__device__ __forceinline__ void run_stage(const Ctx& c, Frags (&f)[4], f32x16 (&acc)[2]) {
  const bool last = (J == 15) && !c.more;
  if (!last) {
    __builtin_amdgcn_s_barrier();
    if constexpr (J < 15) {
      HALF(J, 1, c.xb)
      HALF(J + 1, 0, c.xb)
    } else {
      HALF(0, 0, c.xbn)
    }
  } else {
    WAITF(9, f[0]); MFMA(f[0], 0); WAITF(6, f[1]); MFMA(f[1], 1); WAITF(3, f[2]); MFMA(f[2], 2); WAITF(0, f[3]); MFMA(f[3], 3);
  }
  SB;
  if constexpr (J < 15) run_stage<J + 1>(c, f, acc);
}

__global__ __launch_bounds__(512, 1)
void conv_gemm_kernel(const __hip_bfloat16* __restrict__ x, const __hip_bfloat16* __restrict__ w,
                      const __hip_bfloat16* __restrict__ bias, float* __restrict__ y,
                      float2* __restrict__ part) {
  extern __shared__ __attribute__((aligned(16))) char sm[];
  const int tid = threadIdx.x, lane = tid & 63;
  const int wid8 = __builtin_amdgcn_readfirstlane(tid >> 6);
  const int wq = wid8 & 3;
  const int bid = blockIdx.x;
  const int nt = bid & 7, rest = bid >> 3;
  const int b = rest >> 4, mt = rest & 15;
  const int t0 = 2 * mt * BM;
  const int r = lane & 31, h = lane >> 5;

  Ctx c;
  c.sm = sm; c.wid = wq;
  if (wid8 >= 4) {
    const char* xb = (const char*)(x + (size_t)b * T_ * D_);
    c.wsrc = (const char*)(w + (size_t)((wq >> 1) * 256 + (wq & 1) * 32 + (lane >> 2)) * D_ + nt * BN + (lane & 3) * 8);
    #pragma unroll
    for (int i = 0; i < 9; ++i) {
      int q = wq + 4 * i;
      int e = q * 64 + lane;
      int p = e / (8 * XR); int rem = e - p * (8 * XR);
      int ch = rem / XR, rl = rem - ch * XR;
      int xrow = t0 + 2 * rl + p; xrow = xrow < T_ ? xrow : T_ - 1;
      c.xsrc[i] = xb + (size_t)xrow * 512 + ch * 16;
    }
    c.xdst = 0;
    load_x<0>(c); load_x<1>(c); load_x<2>(c); load_x<3>(c); load_x<4>(c);
    load_x<5>(c); load_x<6>(c); load_x<7>(c); load_x<8>(c);
    load_w<0>(c); load_w<1>(c); load_w<2>(c); load_w<3>(c); load_w<4>(c); load_w<5>(c); load_w<6>(c);
    asm volatile("s_waitcnt vmcnt(12)" ::: "memory");
    __builtin_amdgcn_s_barrier();
    #pragma unroll 1
    for (int cb = 0; cb < 4; ++cb) {
      c.more = cb < 3;
      c.xdst = ((cb + 1) & 1) * XBLK;
      #pragma unroll
      for (int i = 0; i < 9; ++i) c.xsrc[i] += 128;
      run_loader<0>(c);
      c.wsrc += 64 * 512;
    }
    return;
  }
  unsigned warm = 0;
  unsigned wx[8];
  {
    const char* xbw = (const char*)(x + (size_t)b * T_ * D_);
    int ra = t0 + tid; ra = ra < T_ ? ra : T_ - 1;
    int rb = t0 + 256 + (tid & 31); rb = rb < T_ ? rb : T_ - 1;
    #pragma unroll
    for (int i = 0; i < 4; ++i) {
      wx[2 * i] = *(const unsigned*)(xbw + (size_t)ra * 512 + i * 128);
      wx[2 * i + 1] = *(const unsigned*)(xbw + (size_t)rb * 512 + i * 128);
    }
  }
  if (tid < 248) warm = *(const unsigned*)(w + (size_t)(rest * 248 + tid) * D_ + nt * BN);
  const unsigned smb = (unsigned)(size_t)sm;
  c.xb = smb + h * (XR * 16) + (wq * 32 + r) * 16; c.xbn = c.xb + XBLK;
  c.wb = smb + 2 * XBLK + (8 * h + ((lane & 15) >> 2)) * 64 + (((lane >> 4) & 1) * 16 + (lane & 3) * 4) * 2;
  __builtin_amdgcn_s_barrier();
  f32x16 acc2[2] = {};
  Frags f[4];
  read_frag<0, 0, 0>(f[0], c.xb, c.wb); read_frag<0, 0, 1>(f[1], c.xb, c.wb);
  read_frag<0, 0, 2>(f[2], c.xb, c.wb); read_frag<0, 0, 3>(f[3], c.xb, c.wb);
  #pragma unroll 1
  for (int cb = 0; cb < 4; ++cb) {
    c.more = cb < 3;
    run_stage<0>(c, f, acc2);
    unsigned u = c.xb; c.xb = c.xbn; c.xbn = u;
  }
  f32x16 acc = acc2[0] + acc2[1];
  asm volatile("" :: "v"(warm), "v"(wx[0]), "v"(wx[1]), "v"(wx[2]), "v"(wx[3]), "v"(wx[4]), "v"(wx[5]), "v"(wx[6]), "v"(wx[7]));

  __syncthreads();
  const int col = nt * BN + r;
  const int row0 = mt * BM + wq * 32;
  int cnt = L_ - row0; cnt = cnt > 32 ? 32 : (cnt < 0 ? 0 : cnt);
  int cnt_wg = L_ - mt * BM; cnt_wg = cnt_wg > BM ? BM : cnt_wg;
  float* st = (float*)(sm + 32768);
  {
    const float bv = __bfloat162float(bias[col]);
    float* yp = y + ((size_t)b * L_ + row0 + 4 * h) * D_ + col;
    float sum = 0.f;
    #pragma unroll
    for (int i = 0; i < 16; ++i) {
      int rr = (i & 3) + 8 * (i >> 2) + 4 * h;
      float v = acc[i] + bv; acc[i] = v;
      if (rr < cnt) { sum += v; yp[((i & 3) + 8 * (i >> 2)) * D_] = v; }
    }
    sum += __shfl_xor(sum, 32);
    if (h == 0) st[wq * 32 + r] = sum;
  }
  __syncthreads();
  float mean = 0.f;
  {
    mean = (st[r] + st[32 + r] + st[64 + r] + st[96 + r]) / (float)cnt_wg;
    float m2 = 0.f;
    #pragma unroll
    for (int i = 0; i < 16; ++i) {
      int rr = (i & 3) + 8 * (i >> 2) + 4 * h;
      float d = acc[i] - mean;
      m2 += (rr < cnt) ? d * d : 0.f;
    }
    m2 += __shfl_xor(m2, 32);
    if (h == 0) st[128 + wq * 32 + r] = m2;
  }
  __syncthreads();
  if (tid < 32)
    part[((size_t)b * MT + mt) * D_ + col] = make_float2(mean, st[128 + r] + st[160 + r] + st[192 + r] + st[224 + r]);
}

__global__ __launch_bounds__(256)
void norm_gelu_kernel(const float* __restrict__ y, const float2* __restrict__ part,
                      const __hip_bfloat16* __restrict__ gamma, const __hip_bfloat16* __restrict__ beta,
                      __hip_bfloat16* __restrict__ out) {
  __shared__ float s_scale[256], s_shift[256];
  const int tid = threadIdx.x;
  const int b = blockIdx.y;
  const int rows_per = 8;
  const int r0 = blockIdx.x * rows_per;
  const int c4 = (tid & 63) * 4;
  float4 v[2];
  #pragma unroll
  for (int k = 0; k < 2; ++k) {
    int rr = r0 + (tid >> 6) + 4 * k; rr = rr < L_ ? rr : L_ - 1;
    v[k] = *(const float4*)(y + ((size_t)b * L_ + rr) * D_ + c4);
  }
  {
    float2 pm[16];
    #pragma unroll
    for (int i = 0; i < 16; ++i) pm[i] = part[((size_t)b * MT + i) * D_ + tid];
    float s = 0.f;
    #pragma unroll
    for (int i = 0; i < 15; ++i) s += pm[i].x;
    const float nl = (float)(L_ - 15 * BM);
    float mean = (s * (float)BM + pm[15].x * nl) * (1.f / (float)L_);
    float m2 = 0.f;
    #pragma unroll
    for (int i = 0; i < 15; ++i) { float d = pm[i].x - mean; m2 += pm[i].y + (float)BM * d * d; }
    { float d = pm[15].x - mean; m2 += pm[15].y + nl * d * d; }
    float var = m2 * (1.f / (float)L_);
    float rs = rsqrtf(var + 1e-5f);
    float g = __bfloat162float(gamma[tid]), be = __bfloat162float(beta[tid]);
    s_scale[tid] = rs * g;
    s_shift[tid] = be - mean * rs * g;
  }
  __syncthreads();
  #pragma unroll
  for (int k = 0; k < 2; ++k) {
    int rr = r0 + (tid >> 6) + 4 * k;
    if (rr >= L_) break;
    float o[4] = {v[k].x, v[k].y, v[k].z, v[k].w};
    ushort4 ov;
    unsigned short* op = (unsigned short*)&ov;
    #pragma unroll
    for (int i = 0; i < 4; ++i) {
      float z = o[i] * s_scale[c4 + i] + s_shift[c4 + i];
      float u = 0.7978845608028654f * (z + 0.044715f * z * z * z);
      float t = tanhf(u);
      float g = 0.5f * z * (1.f + t);
      __hip_bfloat16 hb = __float2bfloat16(g);
      op[i] = *(unsigned short*)&hb;
    }
    *(ushort4*)(out + ((size_t)b * L_ + rr) * D_ + c4) = ov;
  }
}

extern "C" void kernel_launch(void* const* d_in, const int* in_sizes, int n_in,
                              void* d_out, int out_size, void* d_ws, size_t ws_size,
                              hipStream_t stream) {
  const __hip_bfloat16* x = (const __hip_bfloat16*)d_in[0];
  const __hip_bfloat16* w = (const __hip_bfloat16*)d_in[1];
  const __hip_bfloat16* bias = (const __hip_bfloat16*)d_in[2];
  const __hip_bfloat16* gamma = (const __hip_bfloat16*)d_in[3];
  const __hip_bfloat16* beta = (const __hip_bfloat16*)d_in[4];
  __hip_bfloat16* out = (__hip_bfloat16*)d_out;
  float* y = (float*)d_ws;
  float2* part = (float2*)((char*)d_ws + (size_t)2 * L_ * D_ * 4);
  (void)hipFuncSetAttribute((const void*)conv_gemm_kernel, hipFuncAttributeMaxDynamicSharedMemorySize, LDS_BYTES);
  conv_gemm_kernel<<<256, 512, LDS_BYTES, stream>>>(x, w, bias, y, part);
  dim3 g2((L_ + 7) / 8, 2);
  norm_gelu_kernel<<<g2, 256, 0, stream>>>(y, part, gamma, beta, out);
}
```

```cpp
#include <hip/hip_runtime.h>
#include <hip/hip_bf16.h>

typedef __attribute__((ext_vector_type(8))) short bf16x8;
typedef __attribute__((ext_vector_type(4))) short bf16x4;
typedef __attribute__((ext_vector_type(16))) float f32x16;

constexpr int T_ = 4000, D_ = 256, KT = 31, L_ = 1985;
constexpr int BM = 128, BN = 32;
constexpr int MT = 16;
constexpr int XR = 144;
constexpr int XPAR = XR * 128;
constexpr int XBLK = 2 * XPAR;
constexpr int WST = 8192;
constexpr int RW = 8;
constexpr int LDS_BYTES = 2 * XBLK + RW * WST;

struct Frags { bf16x8 a; bf16x4 b0, b1; };

template <int J, int KC>
__device__ __forceinline__ unsigned a_addr(unsigned xbase, int row, int h) {
  int rr = row + J;
  return xbase + rr * 128 + ((((2 * KC) + h) ^ ((rr >> 1) & 7)) << 4);
}
template <int J, int P, int KC>
__device__ __forceinline__ void read_frag(Frags& f, unsigned aa, unsigned wb) {
  constexpr int WO = (J % RW) * WST + P * 4096 + KC * 1024;
  asm volatile(
      "ds_read_b128 %0, %3 offset:%5\n\t"
      "ds_read_b64_tr_b16 %1, %4 offset:%6\n\t"
      "ds_read_b64_tr_b16 %2, %4 offset:%6+256\n\t"
      : "=&v"(f.a), "=&v"(f.b0), "=&v"(f.b1)
      : "v"(aa), "v"(wb), "i"(P * XPAR + J * 16 + KC * 2 * XR * 16), "i"(WO)
      : "memory");
}

struct Ctx {
  char* sm;
  const char* wsrc;
  const char* xsrc[9];
  unsigned xb, xbn, wb;
  int wid, row, h;
  int xdst;
  bool more;
};

template <int V>
__device__ __forceinline__ void load_w(const Ctx& c) {
  constexpr int nb = V / 16, j = V % 16;
  if (j == 15 && c.wid >= 2) return;
  const char* s = c.wsrc + (size_t)(2 * j * 256 + nb * 64) * 512;
  __builtin_amdgcn_global_load_lds((const unsigned*)s, (unsigned*)(c.sm + 2 * XBLK + (j % RW) * WST + c.wid * 2048), 16, 0, 0);
  __builtin_amdgcn_global_load_lds((const unsigned*)(s + 16 * 512), (unsigned*)(c.sm + 2 * XBLK + (j % RW) * WST + c.wid * 2048 + 1024), 16, 0, 0);
}
template <int I>
__device__ __forceinline__ void load_x(const Ctx& c) {
  __builtin_amdgcn_global_load_lds((const unsigned*)(c.xsrc[I]), (unsigned*)(c.sm + c.xdst + (c.wid + 4 * I) * 1024), 16, 0, 0);
}

template <int J>
__device__ __forceinline__ void run_loader(const Ctx& c) {
  const bool last = (J == 15) && !c.more;
  if (!last) {
    if constexpr (J < 9) asm volatile("s_waitcnt vmcnt(8)" ::: "memory");
    else { if (c.more) asm volatile("s_waitcnt vmcnt(8)" ::: "memory"); else asm volatile("s_waitcnt vmcnt(0)" ::: "memory"); }
    __builtin_amdgcn_s_barrier();
    if constexpr (J + 7 < 16) load_w<J + 7>(c);
    else { if (c.more) load_w<J + 7>(c); }
    if constexpr (J < 9) { if (c.more) load_x<J>(c); }
  }
  if constexpr (J < 15) run_loader<J + 1>(c);
}

#define MFMA(F, K) acc[(K) & 1] = __builtin_amdgcn_mfma_f32_32x32x16_bf16(F.a, __builtin_shufflevector(F.b0, F.b1, 0, 1, 2, 3, 4, 5, 6, 7), acc[(K) & 1], 0, 0, 0)
#define WAITF(N, F) asm volatile("s_waitcnt lgkmcnt(" #N ")" : "+v"(F.a), "+v"(F.b0), "+v"(F.b1) :: "memory")
#define SB __builtin_amdgcn_sched_barrier(0)
#define STEP(KC, NJ, NP, XB) { WAITF(9, f[KC]); MFMA(f[KC], KC); SB; read_frag<NJ, NP, KC>(f[KC], XB, c.wb); }
#define HALF(NJ, NP, XB) STEP(0, NJ, NP, XB) STEP(1, NJ, NP, XB) STEP(2, NJ, NP, XB) STEP(3, NJ, NP, XB)

template <int J>
__device__ __forceinline__ void run_stage(const Ctx& c, Frags (&f)[4], f32x16 (&acc)[2]) {
  const bool last = (J == 15) && !c.more;
  if (!last) {
    __builtin_amdgcn_s_barrier();
    if constexpr (J < 15) {
      HALF(J, 1, c.xb)
      HALF(J + 1, 0, c.xb)
    } else {
      HALF(0, 0, c.xbn)
    }
  } else {
    WAITF(9, f[0]); MFMA(f[0], 0); WAITF(6, f[1]); MFMA(f[1], 1); WAITF(3, f[2]); MFMA(f[2], 2); WAITF(0, f[3]); MFMA(f[3], 3);
  }
  SB;
  if constexpr (J < 15) run_stage<J + 1>(c, f, acc);
}

__global__ __launch_bounds__(512, 1)
void conv_gemm_kernel(const __hip_bfloat16* __restrict__ x, const __hip_bfloat16* __restrict__ w,
                      const __hip_bfloat16* __restrict__ bias, float* __restrict__ y,
                      float2* __restrict__ part) {
  extern __shared__ __attribute__((aligned(16))) char sm[];
  const int tid = threadIdx.x, lane = tid & 63;
  const int wid8 = __builtin_amdgcn_readfirstlane(tid >> 6);
  const int wq = wid8 & 3;
  const int bid = blockIdx.x;
  const int nt = bid & 7, rest = bid >> 3;
  const int b = rest >> 4, mt = rest & 15;
  const int t0 = 2 * mt * BM;
  const int r = lane & 31, h = lane >> 5;

  Ctx c;
  c.sm = sm; c.wid = wq;
  if (wid8 >= 4) {
    const char* xb = (const char*)(x + (size_t)b * T_ * D_);
    c.wsrc = (const char*)(w + (size_t)((wq >> 1) * 256 + (wq & 1) * 32 + (lane >> 2)) * D_ + nt * BN + (lane & 3) * 8);
    #pragma unroll
    for (int i = 0; i < 9; ++i) {
      int q = wq + 4 * i;
      int e = q * 64 + lane;
      int p = e / (8 * XR); int rem = e - p * (8 * XR);
      int ch = rem / XR, rl = rem - ch * XR;
      int xrow = t0 + 2 * rl + p; xrow = xrow < T_ ? xrow : T_ - 1;
      c.xsrc[i] = xb + (size_t)xrow * 512 + ch * 16;
    }
    c.xdst = 0;
    load_x<0>(c); load_x<1>(c); load_x<2>(c); load_x<3>(c); load_x<4>(c);
    load_x<5>(c); load_x<6>(c); load_x<7>(c); load_x<8>(c);
    load_w<0>(c); load_w<1>(c); load_w<2>(c); load_w<3>(c); load_w<4>(c); load_w<5>(c); load_w<6>(c);
    asm volatile("s_waitcnt vmcnt(12)" ::: "memory");
    __builtin_amdgcn_s_barrier();
    #pragma unroll 1
    for (int cb = 0; cb < 4; ++cb) {
      c.more = cb < 3;
      c.xdst = ((cb + 1) & 1) * XBLK;
      #pragma unroll
      for (int i = 0; i < 9; ++i) c.xsrc[i] += 128;
      run_loader<0>(c);
      c.wsrc += 64 * 512;
    }
    return;
  }
  unsigned warm = 0;
  if (tid < 248) warm = *(const unsigned*)(w + (size_t)(rest * 248 + tid) * D_ + nt * BN);
  const unsigned smb = (unsigned)(size_t)sm;
  c.xb = smb + h * (XR * 16) + (wq * 32 + r) * 16; c.xbn = c.xb + XBLK;
  c.wb = smb + 2 * XBLK + (8 * h + ((lane & 15) >> 2)) * 64 + (((lane >> 4) & 1) * 16 + (lane & 3) * 4) * 2;
  __builtin_amdgcn_s_barrier();
  f32x16 acc2[2] = {};
  Frags f[4];
  read_frag<0, 0, 0>(f[0], c.xb, c.wb); read_frag<0, 0, 1>(f[1], c.xb, c.wb);
  read_frag<0, 0, 2>(f[2], c.xb, c.wb); read_frag<0, 0, 3>(f[3], c.xb, c.wb);
  #pragma unroll 1
  for (int cb = 0; cb < 4; ++cb) {
    c.more = cb < 3;
    run_stage<0>(c, f, acc2);
    unsigned u = c.xb; c.xb = c.xbn; c.xbn = u;
  }
  f32x16 acc = acc2[0] + acc2[1];
  asm volatile("" :: "v"(warm));

  __syncthreads();
  const int col = nt * BN + r;
  const int row0 = mt * BM + wq * 32;
  int cnt = L_ - row0; cnt = cnt > 32 ? 32 : (cnt < 0 ? 0 : cnt);
  int cnt_wg = L_ - mt * BM; cnt_wg = cnt_wg > BM ? BM : cnt_wg;
  float* st = (float*)(sm + 32768);
  {
    const float bv = __bfloat162float(bias[col]);
    float* yp = y + ((size_t)b * L_ + row0 + 4 * h) * D_ + col;
    float sum = 0.f;
    #pragma unroll
    for (int i = 0; i < 16; ++i) {
      int rr = (i & 3) + 8 * (i >> 2) + 4 * h;
      float v = acc[i] + bv; acc[i] = v;
      if (rr < cnt) { sum += v; yp[((i & 3) + 8 * (i >> 2)) * D_] = v; }
    }
    sum += __shfl_xor(sum, 32);
    if (h == 0) st[wq * 32 + r] = sum;
  }
  __syncthreads();
  float mean = 0.f;
  {
    mean = (st[r] + st[32 + r] + st[64 + r] + st[96 + r]) / (float)cnt_wg;
    float m2 = 0.f;
    #pragma unroll
    for (int i = 0; i < 16; ++i) {
      int rr = (i & 3) + 8 * (i >> 2) + 4 * h;
      float d = acc[i] - mean;
      m2 += (rr < cnt) ? d * d : 0.f;
    }
    m2 += __shfl_xor(m2, 32);
    if (h == 0) st[128 + wq * 32 + r] = m2;
  }
  __syncthreads();
  if (tid < 32)
    part[((size_t)b * MT + mt) * D_ + col] = make_float2(mean, st[128 + r] + st[160 + r] + st[192 + r] + st[224 + r]);
}

__global__ __launch_bounds__(256)
void norm_gelu_kernel(const float* __restrict__ y, const float2* __restrict__ part,
                      const __hip_bfloat16* __restrict__ gamma, const __hip_bfloat16* __restrict__ beta,
                      __hip_bfloat16* __restrict__ out) {
  __shared__ float s_scale[256], s_shift[256];
  const int tid = threadIdx.x;
  const int b = blockIdx.y;
  const int rows_per = 8;
  const int r0 = blockIdx.x * rows_per;
  const int c4 = (tid & 63) * 4;
  float4 v[2];
  #pragma unroll
  for (int k = 0; k < 2; ++k) {
    int rr = r0 + (tid >> 6) + 4 * k; rr = rr < L_ ? rr : L_ - 1;
    v[k] = *(const float4*)(y + ((size_t)b * L_ + rr) * D_ + c4);
  }
  {
    float2 pm[16];
    #pragma unroll
    for (int i = 0; i < 16; ++i) pm[i] = part[((size_t)b * MT + i) * D_ + tid];
    float s = 0.f;
    #pragma unroll
    for (int i = 0; i < 15; ++i) s += pm[i].x;
    const float nl = (float)(L_ - 15 * BM);
    float mean = (s * (float)BM + pm[15].x * nl) * (1.f / (float)L_);
    float m2 = 0.f;
    #pragma unroll
    for (int i = 0; i < 15; ++i) { float d = pm[i].x - mean; m2 += pm[i].y + (float)BM * d * d; }
    { float d = pm[15].x - mean; m2 += pm[15].y + nl * d * d; }
    float var = m2 * (1.f / (float)L_);
    float rs = rsqrtf(var + 1e-5f);
    float g = __bfloat162float(gamma[tid]), be = __bfloat162float(beta[tid]);
    s_scale[tid] = rs * g;
    s_shift[tid] = be - mean * rs * g;
  }
  __syncthreads();
  #pragma unroll
  for (int k = 0; k < 2; ++k) {
    int rr = r0 + (tid >> 6) + 4 * k;
    if (rr >= L_) break;
    float o[4] = {v[k].x, v[k].y, v[k].z, v[k].w};
    ushort4 ov;
    unsigned short* op = (unsigned short*)&ov;
    #pragma unroll
    for (int i = 0; i < 4; ++i) {
      float z = o[i] * s_scale[c4 + i] + s_shift[c4 + i];
      float u = 0.7978845608028654f * (z + 0.044715f * z * z * z);
      float t = tanhf(u);
      float g = 0.5f * z * (1.f + t);
      __hip_bfloat16 hb = __float2bfloat16(g);
      op[i] = *(unsigned short*)&hb;
    }
    *(ushort4*)(out + ((size_t)b * L_ + rr) * D_ + c4) = ov;
  }
}

extern "C" void kernel_launch(void* const* d_in, const int* in_sizes, int n_in,
                              void* d_out, int out_size, void* d_ws, size_t ws_size,
                              hipStream_t stream) {
  const __hip_bfloat16* x = (const __hip_bfloat16*)d_in[0];
  const __hip_bfloat16* w = (const __hip_bfloat16*)d_in[1];
  const __hip_bfloat16* bias = (const __hip_bfloat16*)d_in[2];
  const __hip_bfloat16* gamma = (const __hip_bfloat16*)d_in[3];
  const __hip_bfloat16* beta = (const __hip_bfloat16*)d_in[4];
  __hip_bfloat16* out = (__hip_bfloat16*)d_out;
  float* y = (float*)d_ws;
  float2* part = (float2*)((char*)d_ws + (size_t)2 * L_ * D_ * 4);
  (void)hipFuncSetAttribute((const void*)conv_gemm_kernel, hipFuncAttributeMaxDynamicSharedMemorySize, LDS_BYTES);
  conv_gemm_kernel<<<256, 512, LDS_BYTES, stream>>>(x, w, bias, y, part);
  dim3 g2((L_ + 7) / 8, 2);
  norm_gelu_kernel<<<g2, 256, 0, stream>>>(y, part, gamma, beta, out);
}
```
